```python
import torch

def _softmax(x, axis=-1):
    return torch.softmax(x, dim=axis)
def _gelu(x):
    return torch.nn.functional.gelu(x, approximate="tanh")
def _silu(x):
    return torch.nn.functional.silu(x)
def _sigmoid(x):
    return torch.sigmoid(x)
def _layernorm(x, axis=-1, eps=1e-5):
    m = x.mean(dim=axis, keepdim=True)
    v = x.var(dim=axis, keepdim=True, unbiased=False)
    return (x - m) / torch.sqrt(v + eps)
def _scatter_add(tgt, idx, src, axis=0):
    return tgt.clone().index_add_(axis, idx, src)
def _segment_sum(src, seg, n):
    out = torch.zeros((n,) + src.shape[1:], dtype=src.dtype)
    return out.index_add_(0, seg, src)
def _pad(x, pw):
    flat = tuple(v for pair in reversed(pw) for v in pair)
    return torch.nn.functional.pad(x, flat)

kernel_name = 'recurrent_scan__compressed_kv_cla__glu_gated_geglu__decode'

def setup_inputs():

    _w = (lambda a: (lambda t: t.to(torch.bfloat16) if t.is_floating_point() else t)(torch.as_tensor(a)))
    def _A_setup():
        B, H, Sq, Sk, D, M = 4, 32, 1, 4096, 128, 576
        Dc = 4 * D
        return {
            "x": _w((lambda *s: torch.randn(*s, dtype=torch.bfloat16) * 0.05)(B, Sq, M)),
            "c_kv": _w((lambda *s: torch.randn(*s, dtype=torch.bfloat16) * 1.6)(B, Sk, Dc)),
            "w_q": _w((lambda *s: torch.randn(*s, dtype=torch.bfloat16) * 1.6)(M, H, D) * (M ** -0.5)),
            "w_uk": _w((lambda *s: torch.randn(*s, dtype=torch.bfloat16) * 1.6)(Dc, H, D) * (Dc ** -0.5)),
            "w_uv": _w((lambda *s: torch.randn(*s, dtype=torch.bfloat16) * 1.6)(Dc, H, D) * (Dc ** -0.5)),
            "w_o": _w((lambda *s: torch.randn(*s, dtype=torch.bfloat16) * 1.6)(H, D, M) * ((H * D) ** -0.5)),
        }
    _A_in = _A_setup()

    def _B_setup():
        B, S, Dm, Df = 4, 1, 576, 4608
        return {
            "x": _w((lambda *s: torch.randn(*s, dtype=torch.bfloat16) * 0.05)(B, S, Dm)),
            "w_gate": _w((lambda *s: torch.randn(*s, dtype=torch.bfloat16) * 0.2)(Dm, Df) * (Dm ** -0.5)),
            "w_up": _w((lambda *s: torch.randn(*s, dtype=torch.bfloat16) * 0.2)(Dm, Df) * (Dm ** -0.5)),
            "w_down": _w((lambda *s: torch.randn(*s, dtype=torch.bfloat16) * 0.2)(Df, Dm) * (Df ** -0.5)),
        }
    _B_in = _B_setup()

    return {
        "A_x": _A_in["x"],
        "A_c_kv": _A_in["c_kv"],
        "A_w_q": _A_in["w_q"],
        "A_w_uk": _A_in["w_uk"],
        "A_w_uv": _A_in["w_uv"],
        "A_w_o": _A_in["w_o"],
        "B_x": _B_in["x"],
        "B_w_gate": _B_in["w_gate"],
        "B_w_up": _B_in["w_up"],
        "B_w_down": _B_in["w_down"],
    }

def reference(A_x, A_c_kv, A_w_q, A_w_uk, A_w_uv, A_w_o, B_x, B_w_gate, B_w_up, B_w_down):
    def _A_ref(x, c_kv, w_q, w_uk, w_uv, w_o):
        B, Sq, M = x.shape[0], x.shape[1], x.shape[2]
        Sk = c_kv.shape[1]
        H, D = w_q.shape[1], w_q.shape[2]
        q = torch.einsum("bqm,mhd->bhqd", x, w_q)
        k = torch.einsum("bkc,chd->bhkd", c_kv, w_uk)
        v = torch.einsum("bkc,chd->bhkd", c_kv, w_uv)
        scores = torch.einsum("bhqd,bhkd->bhqk", q, k) * (D ** -0.5)
        off = Sk - Sq
        qi = torch.arange(Sq)[:, None]
        kj = torch.arange(Sk)[None, :]
        cmask = kj <= (qi + off)
        scores = torch.where(cmask[None, None], scores, -1e9)
        p = _softmax(scores, axis=-1)
        ctx = torch.einsum("bhqk,bhkd->bhqd", p, v)
        return torch.einsum("bhqd,hdm->bqm", ctx, w_o)

    def _B_ref(x, w_gate, w_up, w_down):
        g = torch.einsum("bsd,df->bsf", x, w_gate)
        u = torch.einsum("bsd,df->bsf", x, w_up)
        h = _gelu(g) * u
        return torch.einsum("bsf,fd->bsd", h, w_down)

    x = A_x
    xn = _layernorm(x, axis=-1)
    s = _A_ref(xn, A_c_kv, A_w_q, A_w_uk, A_w_uv, A_w_o)
    g = _silu(_B_ref(xn, B_w_gate, B_w_up, B_w_down))
    return x + s * g
```

```cpp
#include <hip/hip_runtime.h>
#include <hip/hip_bf16.h>
typedef unsigned short u16;
typedef __attribute__((ext_vector_type(4))) unsigned int u32x4;
typedef __attribute__((ext_vector_type(4))) float f32x4;
typedef __attribute__((ext_vector_type(16))) float f32x16;
typedef __attribute__((ext_vector_type(8))) __bf16 bf16x8;

#define NB 4
#define NH 32
#define SK 4096
#define DH 128
#define DM 576
#define DC 512
#define DF 4608

__device__ __forceinline__ float bf2f(u16 v) { return __uint_as_float(((unsigned)v) << 16); }
__device__ __forceinline__ float lo2f(unsigned v) { return __uint_as_float(v << 16); }
__device__ __forceinline__ float hi2f(unsigned v) { return __uint_as_float(v & 0xffff0000u); }
__device__ __forceinline__ u16 f2bf(float f) {
    unsigned u = __float_as_uint(f);
    u += 0x7fffu + ((u >> 16) & 1);
    return (u16)(u >> 16);
}
__device__ __forceinline__ float rbf(float f) { return bf2f(f2bf(f)); }
__device__ __forceinline__ float wave_sum(float v) {
    for (int o = 32; o > 0; o >>= 1) v += __shfl_xor(v, o);
    return v;
}
__device__ __forceinline__ float wave_max(float v) {
    for (int o = 32; o > 0; o >>= 1) v = fmaxf(v, __shfl_xor(v, o));
    return v;
}

__device__ __forceinline__ void do_ln(const u16* x, float* xn) {
    int w = threadIdx.x >> 6, l = threadIdx.x & 63;
    if (w < NB) {
        float v[9]; float s = 0;
        for (int i = 0; i < 9; i++) { v[i] = bf2f(x[w * DM + l + 64 * i]); s += v[i]; }
        float m = rbf(wave_sum(s) * (1.0f / DM));
        float mf = wave_sum(s) * (1.0f / DM);
        float q = 0;
        for (int i = 0; i < 9; i++) { float d = v[i] - mf; q += d * d; }
        float var = rbf(wave_sum(q) * (1.0f / DM));
        float den = rbf(sqrtf(rbf(var + 1e-5f)));
        for (int i = 0; i < 9; i++) xn[w * DM + l + 64 * i] = rbf(rbf(v[i] - m) / den);
    }
}

__global__ __launch_bounds__(256) void k1(const u16* x, const u16* wq, const u16* wuk, const u16* wg, const u16* wu,
                                          u16* qabs, float* hbuf) {
    __shared__ float xn[NB * DM];
    __shared__ float red[4][NB * DH];
    __shared__ float qs[NB * DH];
    do_ln(x, xn);
    __syncthreads();
    int t = threadIdx.x;
    if (blockIdx.x < 256) {
        int h = blockIdx.x >> 3, cs = blockIdx.x & 7;
        int d8 = t & 15, mr = t >> 4;
        float acc[NB][8] = {};
        for (int m = mr; m < DM; m += 16) {
            u32x4 w = *(const u32x4*)(wq + ((size_t)m * NH + h) * DH + d8 * 8);
            float wf[8] = {lo2f(w.x), hi2f(w.x), lo2f(w.y), hi2f(w.y), lo2f(w.z), hi2f(w.z), lo2f(w.w), hi2f(w.w)};
            for (int b = 0; b < NB; b++) { float xv = xn[b * DM + m]; for (int j = 0; j < 8; j++) acc[b][j] += xv * wf[j]; }
        }
        for (int b = 0; b < NB; b++) for (int j = 0; j < 8; j++) {
            float v = acc[b][j]; v += __shfl_xor(v, 16); v += __shfl_xor(v, 32);
            if ((t & 63) < 16) red[t >> 6][b * DH + d8 * 8 + j] = v;
        }
        __syncthreads();
        for (int i = t; i < NB * DH; i += 256) qs[i] = rbf(red[0][i] + red[1][i] + red[2][i] + red[3][i]);
        __syncthreads();
        int c = cs * 64 + (t >> 2), qd = t & 3;
        float a[NB] = {};
        for (int i = 0; i < 4; i++) {
            u32x4 w = *(const u32x4*)(wuk + ((size_t)c * NH + h) * DH + qd * 32 + i * 8);
            float wf[8] = {lo2f(w.x), hi2f(w.x), lo2f(w.y), hi2f(w.y), lo2f(w.z), hi2f(w.z), lo2f(w.w), hi2f(w.w)};
            for (int b = 0; b < NB; b++) for (int j = 0; j < 8; j++) a[b] += qs[b * DH + qd * 32 + i * 8 + j] * wf[j];
        }
        for (int b = 0; b < NB; b++) {
            float v = a[b]; v += __shfl_xor(v, 1); v += __shfl_xor(v, 2);
            if (qd == 0) qabs[((size_t)b * NH + h) * DC + c] = f2bf(v * (0.08838834764831845f * 1.4426950408889634f));
        }
    } else {
        int fb = blockIdx.x - 256;
        int f8 = t & 3, mr = t >> 2;
        float ag[NB][8] = {}, au[NB][8] = {};
        for (int m = mr; m < DM; m += 64) {
            u32x4 w = *(const u32x4*)(wg + (size_t)m * DF + fb * 32 + f8 * 8);
            u32x4 w2 = *(const u32x4*)(wu + (size_t)m * DF + fb * 32 + f8 * 8);
            float wf[8] = {lo2f(w.x), hi2f(w.x), lo2f(w.y), hi2f(w.y), lo2f(w.z), hi2f(w.z), lo2f(w.w), hi2f(w.w)};
            float uf[8] = {lo2f(w2.x), hi2f(w2.x), lo2f(w2.y), hi2f(w2.y), lo2f(w2.z), hi2f(w2.z), lo2f(w2.w), hi2f(w2.w)};
            for (int b = 0; b < NB; b++) { float xv = xn[b * DM + m]; for (int j = 0; j < 8; j++) { ag[b][j] += xv * wf[j]; au[b][j] += xv * uf[j]; } }
        }
        __shared__ float rg[4][NB][32], ru[4][NB][32];
        for (int b = 0; b < NB; b++) for (int j = 0; j < 8; j++) {
            float v = ag[b][j], u = au[b][j];
            for (int o = 4; o < 64; o <<= 1) { v += __shfl_xor(v, o); u += __shfl_xor(u, o); }
            if ((t & 63) < 4) { rg[t >> 6][b][f8 * 8 + j] = v; ru[t >> 6][b][f8 * 8 + j] = u; }
        }
        __syncthreads();
        if (t < NB * 32) {
            int b = t >> 5, f = t & 31;
            float g = rbf(rg[0][b][f] + rg[1][b][f] + rg[2][b][f] + rg[3][b][f]);
            float u = rbf(ru[0][b][f] + ru[1][b][f] + ru[2][b][f] + ru[3][b][f]);
            float inner = 0.7978845608028654f * (g + 0.044715f * g * g * g);
            float ge = rbf(0.5f * g * (1.0f + tanhf(inner)));
            hbuf[b * DF + fb * 32 + f] = rbf(ge * u);
        }
    }
}

__global__ __launch_bounds__(256) void k2(const u16* ckv, const u16* qabs, float* sc, const u16* wd, const float* hbuf, float* ypart) {
    int t = threadIdx.x, l = t & 63, w = t >> 6;
    if (blockIdx.x < 256) {
        int b = blockIdx.x >> 6, kc = blockIdx.x & 63;
        f32x4 acc0 = {0,0,0,0}, acc1 = {0,0,0,0};
        const u16* kp = ckv + ((size_t)b * SK + kc * 64 + w * 16 + (l & 15)) * DC + (l >> 4) * 8;
        const u16* q0 = qabs + ((size_t)b * NH + (l & 15)) * DC + (l >> 4) * 8;
        const u16* q1 = q0 + 16 * DC;
        for (int c = 0; c < DC; c += 32) {
            bf16x8 kb = *(const bf16x8*)(kp + c);
            bf16x8 a0 = *(const bf16x8*)(q0 + c);
            bf16x8 a1 = *(const bf16x8*)(q1 + c);
            acc0 = __builtin_amdgcn_mfma_f32_16x16x32_bf16(a0, kb, acc0, 0, 0, 0);
            acc1 = __builtin_amdgcn_mfma_f32_16x16x32_bf16(a1, kb, acc1, 0, 0, 0);
        }
        int key = kc * 64 + w * 16 + (l & 15);
        for (int r = 0; r < 4; r++) {
            int h0 = (l >> 4) * 4 + r;
            sc[((size_t)b * NH + h0) * SK + key] = acc0[r];
            sc[((size_t)b * NH + h0 + 16) * SK + key] = acc1[r];
        }
    } else {
        int kb = blockIdx.x - 256;
        __shared__ float red[3][NB][DM];
        int c8 = t % 72, rg = t / 72;
        if (rg < 3) {
            float acc[NB][8] = {};
            for (int r = rg; r < 128; r += 3) {
                int f = kb * 128 + r;
                u32x4 wv = *(const u32x4*)(wd + (size_t)f * DM + c8 * 8);
                float wf[8] = {lo2f(wv.x), hi2f(wv.x), lo2f(wv.y), hi2f(wv.y), lo2f(wv.z), hi2f(wv.z), lo2f(wv.w), hi2f(wv.w)};
                for (int b = 0; b < NB; b++) { float hv = hbuf[b * DF + f]; for (int j = 0; j < 8; j++) acc[b][j] += hv * wf[j]; }
            }
            for (int b = 0; b < NB; b++) for (int j = 0; j < 8; j++) red[rg][b][c8 * 8 + j] = acc[b][j];
        }
        __syncthreads();
        for (int i = t; i < NB * DM; i += 256) {
            int b = i / DM, m = i % DM;
            ypart[(size_t)kb * NB * DM + i] = red[0][b][m] + red[1][b][m] + red[2][b][m];
        }
    }
}

__global__ __launch_bounds__(256) void k3(const u16* ckv, const float* sc, float* part, float* stats) {
    __shared__ float p[NH][512 + 1];
    int t = threadIdx.x, l = t & 63, w = t >> 6;
    int b = blockIdx.x >> 6, ch = (blockIdx.x >> 3) & 7, cs = blockIdx.x & 7;
    for (int h = w; h < NH; h += 4) {
        float v[8]; float m = -1e30f;
        for (int i = 0; i < 8; i++) { v[i] = sc[((size_t)b * NH + h) * SK + ch * 512 + l + 64 * i]; m = fmaxf(m, v[i]); }
        m = wave_max(m);
        float s = 0;
        for (int i = 0; i < 8; i++) { float e = exp2f(v[i] - m); s += e; p[h][l + 64 * i] = rbf(e); }
        s = wave_sum(s);
        if (l == 0 && cs == 0) { stats[((b * 8 + ch) * NH + h) * 2] = m; stats[((b * 8 + ch) * NH + h) * 2 + 1] = s; }
    }
    __syncthreads();
    int c = cs * 64 + (t & 63);
    float acc[8] = {};
    for (int k = 0; k < 512; k++) {
        float kv = bf2f(ckv[((size_t)b * SK + ch * 512 + k) * DC + c]);
        for (int j = 0; j < 8; j++) acc[j] += p[w * 8 + j][k] * kv;
    }
    for (int j = 0; j < 8; j++) part[(((size_t)b * 8 + ch) * NH + w * 8 + j) * DC + c] = acc[j];
}

__global__ __launch_bounds__(256) void k4(const float* part, const float* stats, const u16* wuv, const u16* wo, float* spart) {
    __shared__ float cl[NB][DC];
    __shared__ float red[16][NB][DH];
    __shared__ float ctx[NB][DH];
    int t = threadIdx.x;
    int h = blockIdx.x >> 3, ms = blockIdx.x & 7;
    for (int i = t; i < NB * DC; i += 256) {
        int b = i / DC, c = i % DC;
        float M = -1e30f;
        for (int ch = 0; ch < 8; ch++) M = fmaxf(M, stats[((b * 8 + ch) * NH + h) * 2]);
        float L = 0, a = 0;
        for (int ch = 0; ch < 8; ch++) {
            float f = exp2f(stats[((b * 8 + ch) * NH + h) * 2] - M);
            L += f * stats[((b * 8 + ch) * NH + h) * 2 + 1];
            a += f * part[(((size_t)b * 8 + ch) * NH + h) * DC + c];
        }
        cl[b][c] = a / L;
    }
    __syncthreads();
    {
        int d8 = t & 15, cr = t >> 4;
        float acc[NB][8] = {};
        for (int c = cr; c < DC; c += 16) {
            u32x4 w = *(const u32x4*)(wuv + ((size_t)c * NH + h) * DH + d8 * 8);
            float wf[8] = {lo2f(w.x), hi2f(w.x), lo2f(w.y), hi2f(w.y), lo2f(w.z), hi2f(w.z), lo2f(w.w), hi2f(w.w)};
            for (int b = 0; b < NB; b++) { float xv = cl[b][c]; for (int j = 0; j < 8; j++) acc[b][j] += xv * wf[j]; }
        }
        for (int b = 0; b < NB; b++) for (int j = 0; j < 8; j++) red[cr][b][d8 * 8 + j] = acc[b][j];
    }
    __syncthreads();
    for (int i = t; i < NB * DH; i += 256) {
        float s = 0; for (int r = 0; r < 16; r++) s += red[r][i / DH][i % DH];
        ctx[i / DH][i % DH] = rbf(s);
    }
    __syncthreads();
    {
        int c8 = t % 9, rg = t / 9;
        float acc[NB][8] = {};
        if (rg < 16) {
            for (int i = 0; i < 8; i++) {
                int d = rg * 8 + i;
                u32x4 w = *(const u32x4*)(wo + ((size_t)h * DH + d) * DM + ms * 72 + c8 * 8);
                float wf[8] = {lo2f(w.x), hi2f(w.x), lo2f(w.y), hi2f(w.y), lo2f(w.z), hi2f(w.z), lo2f(w.w), hi2f(w.w)};
                for (int b = 0; b < NB; b++) { float xv = ctx[b][d]; for (int j = 0; j < 8; j++) acc[b][j] += xv * wf[j]; }
            }
        }
        __syncthreads();
        float* r2 = &red[0][0][0];
        if (rg < 16) for (int b = 0; b < NB; b++) for (int j = 0; j < 8; j++) r2[(rg * NB + b) * 72 + c8 * 8 + j] = acc[b][j];
        __syncthreads();
        for (int i = t; i < NB * 72; i += 256) {
            int b = i / 72, m = i % 72; float s = 0;
            for (int r = 0; r < 16; r++) s += r2[(r * NB + b) * 72 + m];
            spart[((size_t)h * NB + b) * DM + ms * 72 + m] = s;
        }
    }
}

__global__ __launch_bounds__(256) void k5(const u16* x, const float* spart, const float* ypart, u16* out) {
    int i = blockIdx.x * 256 + threadIdx.x;
    if (i >= NB * DM) return;
    float s = 0, y = 0;
    for (int h = 0; h < NH; h++) s += spart[(size_t)h * NB * DM + i];
    for (int k = 0; k < 36; k++) y += ypart[(size_t)k * NB * DM + i];
    s = rbf(s); y = rbf(y);
    float g = rbf(y / (1.0f + __expf(-y)));
    out[i] = f2bf(bf2f(x[i]) + rbf(s * g));
}

extern "C" void kernel_launch(void* const* d_in, const int* in_sizes, int n_in,
                              void* d_out, int out_size, void* d_ws, size_t ws_size,
                              hipStream_t stream) {
    const u16* x = (const u16*)d_in[0]; const u16* ckv = (const u16*)d_in[1];
    const u16* wq = (const u16*)d_in[2]; const u16* wuk = (const u16*)d_in[3];
    const u16* wuv = (const u16*)d_in[4]; const u16* wo = (const u16*)d_in[5];
    const u16* wg = (const u16*)d_in[7]; const u16* wu = (const u16*)d_in[8]; const u16* wd = (const u16*)d_in[9];
    char* ws = (char*)d_ws;
    u16* qabs = (u16*)ws;
    float* hbuf = (float*)(ws + (1 << 20));
    float* sc = (float*)(ws + (2 << 20));
    float* ypart = (float*)(ws + (5 << 20));
    float* part = (float*)(ws + (6 << 20));
    float* stats = (float*)(ws + (9 << 20));
    float* spart = (float*)(ws + (10 << 20));
    k1<<<256 + 144, 256, 0, stream>>>(x, wq, wuk, wg, wu, qabs, hbuf);
    k2<<<256 + 36, 256, 0, stream>>>(ckv, qabs, sc, wd, hbuf, ypart);
    k3<<<256, 256, 0, stream>>>(ckv, sc, part, stats);
    k4<<<256, 256, 0, stream>>>(part, stats, wuv, wo, spart);
    k5<<<9, 256, 0, stream>>>(x, spart, ypart, (u16*)d_out);
}
```

```cpp
#include <hip/hip_runtime.h>
#include <hip/hip_bf16.h>
typedef unsigned short u16;
typedef __attribute__((ext_vector_type(4))) unsigned int u32x4;
typedef __attribute__((ext_vector_type(4))) float f32x4;
typedef __attribute__((ext_vector_type(16))) float f32x16;
typedef __attribute__((ext_vector_type(8))) __bf16 bf16x8;

#define NB 4
#define NH 32
#define SK 4096
#define DH 128
#define DM 576
#define DC 512
#define DF 4608

__device__ __forceinline__ float bf2f(u16 v) { return __uint_as_float(((unsigned)v) << 16); }
__device__ __forceinline__ float lo2f(unsigned v) { return __uint_as_float(v << 16); }
__device__ __forceinline__ float hi2f(unsigned v) { return __uint_as_float(v & 0xffff0000u); }
__device__ __forceinline__ u16 f2bf(float f) {
    unsigned u = __float_as_uint(f);
    u += 0x7fffu + ((u >> 16) & 1);
    return (u16)(u >> 16);
}
__device__ __forceinline__ float rbf(float f) { return bf2f(f2bf(f)); }
__device__ __forceinline__ float wave_sum(float v) {
    for (int o = 32; o > 0; o >>= 1) v += __shfl_xor(v, o);
    return v;
}
__device__ __forceinline__ float wave_max(float v) {
    for (int o = 32; o > 0; o >>= 1) v = fmaxf(v, __shfl_xor(v, o));
    return v;
}

__device__ __forceinline__ void do_ln(const u16* x, float* xn) {
    int w = threadIdx.x >> 6, l = threadIdx.x & 63;
    if (w < NB) {
        float v[9]; float s = 0;
        for (int i = 0; i < 9; i++) { v[i] = bf2f(x[w * DM + l + 64 * i]); s += v[i]; }
        float m = rbf(wave_sum(s) * (1.0f / DM));
        float mf = wave_sum(s) * (1.0f / DM);
        float q = 0;
        for (int i = 0; i < 9; i++) { float d = v[i] - mf; q += d * d; }
        float var = rbf(wave_sum(q) * (1.0f / DM));
        float den = rbf(sqrtf(rbf(var + 1e-5f)));
        for (int i = 0; i < 9; i++) xn[w * DM + l + 64 * i] = rbf(rbf(v[i] - m) / den);
    }
}

__global__ __launch_bounds__(256) void k1(const u16* x, const u16* wq, const u16* wuk, const u16* wg, const u16* wu,
                                          u16* qabs, float* hbuf) {
    __shared__ float xn[NB * DM];
    __shared__ float red[4][NB * DH];
    __shared__ float qs[NB * DH];
    do_ln(x, xn);
    __syncthreads();
    int t = threadIdx.x;
    if (blockIdx.x < 256) {
        int h = blockIdx.x >> 3, cs = blockIdx.x & 7;
        int d8 = t & 15, mr = t >> 4;
        float acc[NB][8] = {};
        for (int m = mr; m < DM; m += 16) {
            u32x4 w = *(const u32x4*)(wq + ((size_t)m * NH + h) * DH + d8 * 8);
            float wf[8] = {lo2f(w.x), hi2f(w.x), lo2f(w.y), hi2f(w.y), lo2f(w.z), hi2f(w.z), lo2f(w.w), hi2f(w.w)};
            for (int b = 0; b < NB; b++) { float xv = xn[b * DM + m]; for (int j = 0; j < 8; j++) acc[b][j] += xv * wf[j]; }
        }
        for (int b = 0; b < NB; b++) for (int j = 0; j < 8; j++) {
            float v = acc[b][j]; v += __shfl_xor(v, 16); v += __shfl_xor(v, 32);
            if ((t & 63) < 16) red[t >> 6][b * DH + d8 * 8 + j] = v;
        }
        __syncthreads();
        for (int i = t; i < NB * DH; i += 256) qs[i] = rbf(red[0][i] + red[1][i] + red[2][i] + red[3][i]);
        __syncthreads();
        int c = cs * 64 + (t >> 2), qd = t & 3;
        float a[NB] = {};
        for (int i = 0; i < 4; i++) {
            u32x4 w = *(const u32x4*)(wuk + ((size_t)c * NH + h) * DH + qd * 32 + i * 8);
            float wf[8] = {lo2f(w.x), hi2f(w.x), lo2f(w.y), hi2f(w.y), lo2f(w.z), hi2f(w.z), lo2f(w.w), hi2f(w.w)};
            for (int b = 0; b < NB; b++) for (int j = 0; j < 8; j++) a[b] += qs[b * DH + qd * 32 + i * 8 + j] * wf[j];
        }
        for (int b = 0; b < NB; b++) {
            float v = a[b]; v += __shfl_xor(v, 1); v += __shfl_xor(v, 2);
            if (qd == 0) qabs[((size_t)b * NH + h) * DC + c] = f2bf(v * (0.08838834764831845f * 1.4426950408889634f));
        }
    } else {
        int fb = blockIdx.x - 256;
        int f8 = t & 3, mr = t >> 2;
        float ag[NB][8] = {}, au[NB][8] = {};
        for (int m = mr; m < DM; m += 64) {
            u32x4 w = *(const u32x4*)(wg + (size_t)m * DF + fb * 32 + f8 * 8);
            u32x4 w2 = *(const u32x4*)(wu + (size_t)m * DF + fb * 32 + f8 * 8);
            float wf[8] = {lo2f(w.x), hi2f(w.x), lo2f(w.y), hi2f(w.y), lo2f(w.z), hi2f(w.z), lo2f(w.w), hi2f(w.w)};
            float uf[8] = {lo2f(w2.x), hi2f(w2.x), lo2f(w2.y), hi2f(w2.y), lo2f(w2.z), hi2f(w2.z), lo2f(w2.w), hi2f(w2.w)};
            for (int b = 0; b < NB; b++) { float xv = xn[b * DM + m]; for (int j = 0; j < 8; j++) { ag[b][j] += xv * wf[j]; au[b][j] += xv * uf[j]; } }
        }
        __shared__ float rg[4][NB][32], ru[4][NB][32];
        for (int b = 0; b < NB; b++) for (int j = 0; j < 8; j++) {
            float v = ag[b][j], u = au[b][j];
            for (int o = 4; o < 64; o <<= 1) { v += __shfl_xor(v, o); u += __shfl_xor(u, o); }
            if ((t & 63) < 4) { rg[t >> 6][b][f8 * 8 + j] = v; ru[t >> 6][b][f8 * 8 + j] = u; }
        }
        __syncthreads();
        if (t < NB * 32) {
            int b = t >> 5, f = t & 31;
            float g = rbf(rg[0][b][f] + rg[1][b][f] + rg[2][b][f] + rg[3][b][f]);
            float u = rbf(ru[0][b][f] + ru[1][b][f] + ru[2][b][f] + ru[3][b][f]);
            float inner = 0.7978845608028654f * (g + 0.044715f * g * g * g);
            float ge = rbf(0.5f * g * (1.0f + tanhf(inner)));
            hbuf[b * DF + fb * 32 + f] = rbf(ge * u);
        }
    }
}

__global__ __launch_bounds__(256) void k2(const u16* ckv, const u16* qabs, float* sc, const u16* wd, const float* hbuf, float* ypart) {
    int t = threadIdx.x, l = t & 63, w = t >> 6;
    if (blockIdx.x < 256) {
        int b = blockIdx.x >> 6, kc = blockIdx.x & 63;
        f32x4 acc0 = {0,0,0,0}, acc1 = {0,0,0,0};
        const u16* kp = ckv + ((size_t)b * SK + kc * 64 + w * 16 + (l & 15)) * DC + (l >> 4) * 8;
        const u16* q0 = qabs + ((size_t)b * NH + (l & 15)) * DC + (l >> 4) * 8;
        const u16* q1 = q0 + 16 * DC;
        for (int c = 0; c < DC; c += 32) {
            bf16x8 kb = *(const bf16x8*)(kp + c);
            bf16x8 a0 = *(const bf16x8*)(q0 + c);
            bf16x8 a1 = *(const bf16x8*)(q1 + c);
            acc0 = __builtin_amdgcn_mfma_f32_16x16x32_bf16(a0, kb, acc0, 0, 0, 0);
            acc1 = __builtin_amdgcn_mfma_f32_16x16x32_bf16(a1, kb, acc1, 0, 0, 0);
        }
        int key = kc * 64 + w * 16 + (l & 15);
        for (int r = 0; r < 4; r++) {
            int h0 = (l >> 4) * 4 + r;
            sc[((size_t)b * NH + h0) * SK + key] = acc0[r];
            sc[((size_t)b * NH + h0 + 16) * SK + key] = acc1[r];
        }
    } else {
        int kb = blockIdx.x - 256;
        __shared__ float red[3][NB][DM];
        int c8 = t % 72, rg = t / 72;
        if (rg < 3) {
            float acc[NB][8] = {};
            for (int r = rg; r < 128; r += 3) {
                int f = kb * 128 + r;
                u32x4 wv = *(const u32x4*)(wd + (size_t)f * DM + c8 * 8);
                float wf[8] = {lo2f(wv.x), hi2f(wv.x), lo2f(wv.y), hi2f(wv.y), lo2f(wv.z), hi2f(wv.z), lo2f(wv.w), hi2f(wv.w)};
                for (int b = 0; b < NB; b++) { float hv = hbuf[b * DF + f]; for (int j = 0; j < 8; j++) acc[b][j] += hv * wf[j]; }
            }
            for (int b = 0; b < NB; b++) for (int j = 0; j < 8; j++) red[rg][b][c8 * 8 + j] = acc[b][j];
        }
        __syncthreads();
        for (int i = t; i < NB * DM; i += 256) {
            int b = i / DM, m = i % DM;
            ypart[(size_t)kb * NB * DM + i] = red[0][b][m] + red[1][b][m] + red[2][b][m];
        }
    }
}

__global__ __launch_bounds__(256) void k3(const u16* ckv, const float* sc, float* part, float* stats) {
    __shared__ float p[NH][512 + 1];
    int t = threadIdx.x, l = t & 63, w = t >> 6;
    int b = blockIdx.x >> 6, ch = (blockIdx.x >> 3) & 7, cs = blockIdx.x & 7;
    for (int h = w; h < NH; h += 4) {
        float v[8]; float m = -1e30f;
        for (int i = 0; i < 8; i++) { v[i] = sc[((size_t)b * NH + h) * SK + ch * 512 + l + 64 * i]; m = fmaxf(m, v[i]); }
        m = wave_max(m);
        float s = 0;
        for (int i = 0; i < 8; i++) { float e = exp2f(v[i] - m); s += e; p[h][l + 64 * i] = rbf(e); }
        s = wave_sum(s);
        if (l == 0 && cs == 0) { stats[((b * 8 + ch) * NH + h) * 2] = m; stats[((b * 8 + ch) * NH + h) * 2 + 1] = s; }
    }
    __syncthreads();
    int c = cs * 64 + (t & 63);
    float acc[8] = {};
    for (int k = 0; k < 512; k++) {
        float kv = bf2f(ckv[((size_t)b * SK + ch * 512 + k) * DC + c]);
        for (int j = 0; j < 8; j++) acc[j] += p[w * 8 + j][k] * kv;
    }
    for (int j = 0; j < 8; j++) part[(((size_t)b * 8 + ch) * NH + w * 8 + j) * DC + c] = acc[j];
}

__global__ __launch_bounds__(256) void k4(const float* part, const float* stats, const u16* wuv, const u16* wo, float* spart) {
    __shared__ float cl[NB][DC];
    __shared__ float red[16][NB][DH];
    __shared__ float ctx[NB][DH];
    int t = threadIdx.x;
    int h = blockIdx.x >> 3, ms = blockIdx.x & 7;
    for (int i = t; i < NB * DC; i += 256) {
        int b = i / DC, c = i % DC;
        float M = -1e30f;
        for (int ch = 0; ch < 8; ch++) M = fmaxf(M, stats[((b * 8 + ch) * NH + h) * 2]);
        float L = 0, a = 0;
        for (int ch = 0; ch < 8; ch++) {
            float f = exp2f(stats[((b * 8 + ch) * NH + h) * 2] - M);
            L += f * stats[((b * 8 + ch) * NH + h) * 2 + 1];
            a += f * part[(((size_t)b * 8 + ch) * NH + h) * DC + c];
        }
        cl[b][c] = a / L;
    }
    __syncthreads();
    {
        int d8 = t & 15, cr = t >> 4;
        float acc[NB][8] = {};
        for (int c = cr; c < DC; c += 16) {
            u32x4 w = *(const u32x4*)(wuv + ((size_t)c * NH + h) * DH + d8 * 8);
            float wf[8] = {lo2f(w.x), hi2f(w.x), lo2f(w.y), hi2f(w.y), lo2f(w.z), hi2f(w.z), lo2f(w.w), hi2f(w.w)};
            for (int b = 0; b < NB; b++) { float xv = cl[b][c]; for (int j = 0; j < 8; j++) acc[b][j] += xv * wf[j]; }
        }
        for (int b = 0; b < NB; b++) for (int j = 0; j < 8; j++) red[cr][b][d8 * 8 + j] = acc[b][j];
    }
    __syncthreads();
    for (int i = t; i < NB * DH; i += 256) {
        float s = 0; for (int r = 0; r < 16; r++) s += red[r][i / DH][i % DH];
        ctx[i / DH][i % DH] = rbf(s);
    }
    __syncthreads();
    {
        int c8 = t % 9, rg = t / 9;
        float acc[NB][8] = {};
        if (rg < 16) {
            for (int i = 0; i < 8; i++) {
                int d = rg * 8 + i;
                u32x4 w = *(const u32x4*)(wo + ((size_t)h * DH + d) * DM + ms * 72 + c8 * 8);
                float wf[8] = {lo2f(w.x), hi2f(w.x), lo2f(w.y), hi2f(w.y), lo2f(w.z), hi2f(w.z), lo2f(w.w), hi2f(w.w)};
                for (int b = 0; b < NB; b++) { float xv = ctx[b][d]; for (int j = 0; j < 8; j++) acc[b][j] += xv * wf[j]; }
            }
        }
        __syncthreads();
        float* r2 = &red[0][0][0];
        if (rg < 16) for (int b = 0; b < NB; b++) for (int j = 0; j < 8; j++) r2[(rg * NB + b) * 72 + c8 * 8 + j] = acc[b][j];
        __syncthreads();
        for (int i = t; i < NB * 72; i += 256) {
            int b = i / 72, m = i % 72; float s = 0;
            for (int r = 0; r < 16; r++) s += r2[(r * NB + b) * 72 + m];
            spart[((size_t)h * NB + b) * DM + ms * 72 + m] = s;
        }
    }
}

__global__ __launch_bounds__(256) void k5(const u16* x, const float* spart, const float* ypart, u16* out) {
    int i = blockIdx.x * 256 + threadIdx.x;
    if (i >= NB * DM) return;
    float s = 0, y = 0;
    for (int h = 0; h < NH; h++) s += spart[(size_t)h * NB * DM + i];
    for (int k = 0; k < 36; k++) y += ypart[(size_t)k * NB * DM + i];
    s = rbf(s); y = rbf(y);
    float g = rbf(y / (1.0f + __expf(-y)));
    out[i] = f2bf(bf2f(x[i]) + rbf(s * g));
}


typedef unsigned long long u64;
__device__ __forceinline__ u64 rt() { return __builtin_amdgcn_s_memrealtime(); }
template<int NT> __device__ __forceinline__ u32x4 ld(const u32x4* p) { return NT ? __builtin_nontemporal_load(p) : *p; }
template<int NT, int MODE>
__global__ __launch_bounds__(512, 2) void k_stream(const u32x4* ckv, const u32x4* wq, const u32x4* wuk, const u32x4* wuv,
                       const u32x4* wo, const u32x4* wg, const u32x4* wu, const u32x4* wd, u64* stamps, unsigned* sink) {
    u64 t0 = rt();
    int t = blockIdx.x * 512 + threadIdx.x;
    const int T = 131072;
    u32x4 acc = {0,0,0,0};
    if (MODE == 0) {
        u32x4 v[26];
        int c = 0;
#pragma unroll
        for (int i = 0; i < 2; i++) v[c++] = ld<NT>(&wq[t + i * T]);
#pragma unroll
        for (int i = 0; i < 2; i++) v[c++] = ld<NT>(&wuk[t + i * T]);
#pragma unroll
        for (int i = 0; i < 8; i++) v[c++] = ld<NT>(&ckv[t + i * T]);
#pragma unroll
        for (int i = 0; i < 2; i++) v[c++] = ld<NT>(&wuv[t + i * T]);
#pragma unroll
        for (int i = 0; i < 2; i++) v[c++] = ld<NT>(&wo[t + i * T]);
#pragma unroll
        for (int i = 0; i < 2; i++) v[c++] = ld<NT>(&wg[t + i * T]);
#pragma unroll
        for (int i = 0; i < 2; i++) v[c++] = ld<NT>(&wu[t + i * T]);
#pragma unroll
        for (int i = 0; i < 2; i++) v[c++] = ld<NT>(&wd[t + i * T]);
        int t1 = t < 32768 ? t : 0, t2 = t < 69632 ? t : 0;
        v[20] = ld<NT>(&wq[2*T + t1]);
        v[21] = ld<NT>(&wo[2*T + t1]);
        v[22] = ld<NT>(&wg[2*T + t2]);
        v[23] = ld<NT>(&wu[2*T + t2]);
        v[24] = ld<NT>(&wd[2*T + t2]);
        __builtin_amdgcn_sched_barrier(0);
#pragma unroll
        for (int i = 0; i < 25; i++) acc ^= v[i];
    } else if (MODE == 1) {
        acc = ld<NT>(&wq[t]);
    } else if (MODE == 2) {
        u32x4 v[8];
#pragma unroll
        for (int i = 0; i < 8; i++) v[i] = ld<NT>(&ckv[t + i * T]);
        __builtin_amdgcn_sched_barrier(0);
#pragma unroll
        for (int i = 0; i < 8; i++) acc ^= v[i];
    }
    unsigned r2 = acc.x ^ acc.y ^ acc.z ^ acc.w;
    if (r2 == 0x12345678) sink[t] = r2;
    __syncthreads();
    u64 t1 = rt();
    if (threadIdx.x == 0) { stamps[blockIdx.x * 2] = t0; stamps[blockIdx.x * 2 + 1] = t1; }
}
template<int WHAT, int AMP>
__global__ void k_spin(const u64* stamps) {
    int t = threadIdx.x;
    u64 s = stamps[t * 2], e = stamps[t * 2 + 1];
    __shared__ u64 ss[256], ee[256];
    ss[t] = s; ee[t] = e;
    __syncthreads();
    if (t == 0) {
        u64 mn = ~0ull, mx = 0, mxs = 0, sum = 0;
        for (int i = 0; i < 256; i++) { mn = ss[i] < mn ? ss[i] : mn; mx = ee[i] > mx ? ee[i] : mx; mxs = ss[i] > mxs ? ss[i] : mxs; sum += ee[i] - ss[i]; }
        u64 d = WHAT == 0 ? mx - mn : WHAT == 1 ? mxs - mn : sum / 256;
        if (WHAT == 2) d = (sum * AMP) / 256; else d *= AMP;
        if (d > 500000) d = 500000;
        u64 t0 = rt();
        while (rt() - t0 < d) __builtin_amdgcn_s_sleep(8);
    }
}
extern "C" void kernel_launch(void* const* d_in, const int* in_sizes, int n_in,
                              void* d_out, int out_size, void* d_ws, size_t ws_size,
                              hipStream_t stream) {
    const u16* x = (const u16*)d_in[0]; const u16* ckv = (const u16*)d_in[1];
    const u16* wq = (const u16*)d_in[2]; const u16* wuk = (const u16*)d_in[3];
    const u16* wuv = (const u16*)d_in[4]; const u16* wo = (const u16*)d_in[5];
    const u16* wg = (const u16*)d_in[7]; const u16* wu = (const u16*)d_in[8]; const u16* wd = (const u16*)d_in[9];
    char* ws = (char*)d_ws;
    u16* qabs = (u16*)ws;
    float* hbuf = (float*)(ws + (1 << 20));
    float* sc = (float*)(ws + (2 << 20));
    float* ypart = (float*)(ws + (5 << 20));
    float* part = (float*)(ws + (6 << 20));
    float* stats = (float*)(ws + (9 << 20));
    float* spart = (float*)(ws + (10 << 20));
    k_stream<0,1><<<256, 512, 0, stream>>>((const u32x4*)ckv, (const u32x4*)wq, (const u32x4*)wuk, (const u32x4*)wuv, (const u32x4*)wo, (const u32x4*)wg, (const u32x4*)wu, (const u32x4*)wd, (u64*)(ws + (12<<20)), (unsigned*)(ws + (13<<20)));
    k1<<<256 + 144, 256, 0, stream>>>(x, wq, wuk, wg, wu, qabs, hbuf);
    k2<<<256 + 36, 256, 0, stream>>>(ckv, qabs, sc, wd, hbuf, ypart);
    k3<<<256, 256, 0, stream>>>(ckv, sc, part, stats);
    k4<<<256, 256, 0, stream>>>(part, stats, wuv, wo, spart);
    k5<<<9, 256, 0, stream>>>(x, spart, ypart, (u16*)d_out);
    k_spin<1,100><<<1, 256, 0, stream>>>((const u64*)(ws + (12<<20)));
}
```

```cpp
#include <hip/hip_runtime.h>
typedef unsigned short u16;
typedef unsigned int u32;
typedef unsigned long long u64;
typedef u32 u32x4 __attribute__((ext_vector_type(4)));
typedef u32 u32x2 __attribute__((ext_vector_type(2)));
typedef float f32x4 __attribute__((ext_vector_type(4)));
typedef float f32x2 __attribute__((ext_vector_type(2)));
typedef float f32x16 __attribute__((ext_vector_type(16)));
typedef __bf16 bf16x8 __attribute__((ext_vector_type(8)));
typedef __bf16 bf16x2 __attribute__((ext_vector_type(2)));
typedef short s16x4 __attribute__((ext_vector_type(4)));
typedef __amdgpu_buffer_rsrc_t rsrc_t;
#define DI __device__ __forceinline__
#define LAS __attribute__((address_space(3)))

#define WS_QA   4096u
#define WS_PART (WS_QA + 262144u)
#define WS_ST   (WS_PART + 4194304u)
#define WS_SP   (WS_ST + 32768u)
#define WS_YP   (WS_SP + 1179648u)
#define WS_END  (WS_YP + 1327104u)
#define WS_DBG  (8u << 20)

#define SPIN_MAX 20000
#define AUX_WQ 0
#define AUX_WUK 2
#define AUX_FFN 2
#define AUX_WUV 2
#define AUX_WO 0
#define KV_NT " nt"
#define DMA_SPLIT 12
#define DMA_WAIT 4
#define ZPRIME 1
#define FFN_DELAY 0
#define AUX_SC1 16
#define OOB 0x80000000u

#define STAMP(i) do { } while (0)

DI float lo2f(u32 v) { return __uint_as_float(v << 16); }
DI float hi2f(u32 v) { return __uint_as_float(v & 0xffff0000u); }
DI u32 pk2(float lo, float hi) { f32x2 v = {lo, hi}; return __builtin_bit_cast(u32, __builtin_convertvector(v, bf16x2)); }
DI float rbf(float f) { return lo2f(pk2(f, 0.f)); }
DI rsrc_t mkrs(const void* p, u32 bytes) { return __builtin_amdgcn_make_buffer_rsrc((void*)p, 0, (int)bytes, 0x00020000); }
template <int AUX> DI u32x4 ldb(rsrc_t rs, u32 voff, u32 soff) { return __builtin_amdgcn_raw_buffer_load_b128(rs, voff, soff, AUX); }
DI void stb(u32x4 v, rsrc_t rs, u32 voff, u32 soff) { __builtin_amdgcn_raw_buffer_store_b128(v, rs, voff, soff, AUX_SC1); }
DI void bar() { asm volatile("s_waitcnt lgkmcnt(0)\n\ts_barrier" ::: "memory"); }
DI void pin() { asm volatile("" ::: "memory"); }
DI f32x4 mf16(u32x4 a, u32x4 b, f32x4 c) { return __builtin_amdgcn_mfma_f32_16x16x32_bf16(__builtin_bit_cast(bf16x8, a), __builtin_bit_cast(bf16x8, b), c, 0, 0, 0); }
DI f32x16 mf32(u32x4 a, u32x4 b, f32x16 c) { return __builtin_amdgcn_mfma_f32_32x32x16_bf16(__builtin_bit_cast(bf16x8, a), __builtin_bit_cast(bf16x8, b), c, 0, 0, 0); }
DI float swap32max(float v) { auto r = __builtin_amdgcn_permlane32_swap(__float_as_uint(v), __float_as_uint(v), false, false); return fmaxf(__uint_as_float(r[0]), __uint_as_float(r[1])); }
DI float swap32sum(float v) { auto r = __builtin_amdgcn_permlane32_swap(__float_as_uint(v), __float_as_uint(v), false, false); return __uint_as_float(r[0]) + __uint_as_float(r[1]); }
DI float swap16max(float v) { auto r = __builtin_amdgcn_permlane16_swap(__float_as_uint(v), __float_as_uint(v), false, false); return fmaxf(__uint_as_float(r[0]), __uint_as_float(r[1])); }
DI float swap16sum(float v) { auto r = __builtin_amdgcn_permlane16_swap(__float_as_uint(v), __float_as_uint(v), false, false); return __uint_as_float(r[0]) + __uint_as_float(r[1]); }
template <int CTRL> DI float dpp(float v) { return __uint_as_float(__builtin_amdgcn_update_dpp(0, __float_as_uint(v), CTRL, 0xf, 0xf, true)); }
DI float wave_sum(float v) {
    v += dpp<0xb1>(v);
    v += dpp<0x4e>(v);
    v += dpp<0x141>(v);
    v += dpp<0x140>(v);
    v = swap16sum(v);
    return swap32sum(v);
}
DI void tr8(const u32x4 (&r)[8], u32x4 (&t)[8]) {
#pragma unroll
    for (int jj = 0; jj < 4; jj++) {
#pragma unroll
        for (int p = 0; p < 4; p++) {
            t[2 * jj][p] = __builtin_amdgcn_perm(r[2 * p + 1][jj], r[2 * p][jj], 0x05040100u);
            t[2 * jj + 1][p] = __builtin_amdgcn_perm(r[2 * p + 1][jj], r[2 * p][jj], 0x07060302u);
        }
    }
}
template <int AUX> DI void ld8(u32x4 (&r)[8], rsrc_t rs, u32 voff, u32 soff, u32 stride) {
#pragma unroll
    for (int i = 0; i < 8; i++) r[i] = ldb<AUX>(rs, voff, soff + i * stride);
}
DI void gemv8(const u32x4 (&r)[8], u32x4 a, f32x4 (&acc)[8]) {
    u32x4 t[8]; tr8(r, t);
#pragma unroll
    for (int j = 0; j < 8; j++) acc[j] = mf16(a, t[j], acc[j]);
}
DI u32 tag12(float v, u32 tg) { return ((__float_as_uint(v) + 0x800u) & 0xfffff000u) | tg; }
DI u32x4 lds128(const char* p) { return *(const u32x4*)p; }

#define XN_STRIDE 1216
#define H_XN   0
#define H_RED  5120
#define H_QB   (H_RED + 20480)
#define H_HB   (H_QB + 1280)
#define H_FIN  (H_HB + 512)

DI void h_role(char* smem, const u16* x, const u16* wq, const u16* wuk, const u16* wg, const u16* wu, const u16* wd,
               u16* out, char* ws) {
    const int tid = threadIdx.x, l = tid & 63, w = __builtin_amdgcn_readfirstlane(tid >> 6);
    const int k = blockIdx.x, xc = k & 7, ii = k >> 3, h = 4 * xc + (ii >> 2), cq = ii & 3;
    const int n = l & 15, g = l >> 4;
    STAMP(0);
    const rsrc_t rws = mkrs(ws, WS_END);
    const u32 E = __hip_atomic_load((const u32*)ws, __ATOMIC_RELAXED, __HIP_MEMORY_SCOPE_AGENT);
    u32x2 xv[3];
    {
        const rsrc_t rx = mkrs(x, 4 * 576 * 2);
        const u32 vo = (w < 4) ? (u32)(w * 1152 + l * 8) : OOB;
        xv[0] = __builtin_amdgcn_raw_buffer_load_b64(rx, vo, 0, 0);
        xv[1] = __builtin_amdgcn_raw_buffer_load_b64(rx, vo, 512, 0);
        xv[2] = __builtin_amdgcn_raw_buffer_load_b64(rx, l < 16 ? vo : OOB, 1024, 0);
    }
    const rsrc_t rq = mkrs(wq, 576 * 4096 * 2);
    const int rot = (9 * cq) >> 1;
    int b0 = w + rot; if (b0 >= 18) b0 -= 18;
    int b1 = w + 8 + rot; if (b1 >= 18) b1 -= 18;
    int b2 = w + 16 + rot; if (b2 >= 18) b2 -= 18;
    const u32 vq = (u32)(8 * g * 8192 + h * 256 + n * 16);
    u32x4 r0[8], r1[8], r2[8];
    ld8<AUX_WQ>(r0, rq, vq, b0 * 262144, 8192);
    ld8<AUX_WQ>(r1, rq, vq, b1 * 262144, 8192);
    ld8<AUX_WQ>(r2, rq, w < 2 ? vq : OOB, b2 * 262144, 8192);
    u32x4 uk[4];
    {
        const rsrc_t ru = mkrs(wuk, 512 * 4096 * 2);
        const u32 vo = (u32)(((128 * cq + 16 * w + n) * 32 + h) * 256 + g * 16);
#pragma unroll
        for (int s = 0; s < 4; s++) uk[s] = ldb<AUX_WUK>(ru, vo + s * 64, 0);
    }
    {
        const u32x4 ones = {~0u, ~0u, ~0u, ~0u};
#pragma unroll
        for (int i = 0; i < 4; i++) stb(ones, rws, (u32)(tid * 16 + i * 8192), WS_PART + (h * 4 + cq) * 32768);
        if (tid < 16) stb(ones, rws, (u32)(tid * 16), WS_ST + h * 1024 + cq * 256);
    }
    pin();
    if (w < 4) {
        float v[12];
#pragma unroll
        for (int i = 0; i < 3; i++) { v[4 * i] = lo2f(xv[i][0]); v[4 * i + 1] = hi2f(xv[i][0]); v[4 * i + 2] = lo2f(xv[i][1]); v[4 * i + 3] = hi2f(xv[i][1]); }
        float s = 0;
#pragma unroll
        for (int i = 0; i < 12; i++) s += v[i];
        const float mf = wave_sum(s) * (1.0f / 576);
        const float m = rbf(mf);
        float q = 0;
#pragma unroll
        for (int i = 0; i < 8; i++) { float d = v[i] - mf; q += d * d; }
        if (l < 16) {
#pragma unroll
            for (int i = 8; i < 12; i++) { float d = v[i] - mf; q += d * d; }
        }
        const float var = rbf(wave_sum(q) * (1.0f / 576));
        const float den = rbf(sqrtf(rbf(var + 1e-5f)));
#pragma unroll
        for (int i = 0; i < 3; i++) {
            u32x2 o;
            o[0] = pk2(rbf(v[4 * i] - m) / den, rbf(v[4 * i + 1] - m) / den);
            o[1] = pk2(rbf(v[4 * i + 2] - m) / den, rbf(v[4 * i + 3] - m) / den);
            if (i < 2 || l < 16) *(u32x2*)(smem + H_XN + w * XN_STRIDE + i * 512 + l * 8) = o;
        }
    }
    bar();
    STAMP(1);
    {
        f32x4 acc[8];
#pragma unroll
        for (int j = 0; j < 8; j++) acc[j] = f32x4{0, 0, 0, 0};
        const char* xa = smem + H_XN + (n >> 2) * XN_STRIDE + g * 16;
        gemv8(r0, lds128(xa + b0 * 64), acc);
        gemv8(r1, lds128(xa + b1 * 64), acc);
        if (w < 2) gemv8(r2, lds128(xa + b2 * 64), acc);
        float* red = (float*)(smem + H_RED) + (w * 4 + g) * 128 + 4 * n;
        *(f32x4*)red = f32x4{acc[0][0], acc[1][0], acc[2][0], acc[3][0]};
        *(f32x4*)(red + 64) = f32x4{acc[4][0], acc[5][0], acc[6][0], acc[7][0]};
    }
    STAMP(2);
    bar();
    {
        const float* red = (const float*)(smem + H_RED) + tid;
        float s = 0;
#pragma unroll
        for (int i = 0; i < 8; i++) s += red[i * 512];
        const int b = tid >> 7, idx = tid & 127, d = 8 * ((idx >> 2) & 15) + 4 * (idx >> 6) + (idx & 3);
        *(u16*)(smem + H_QB + b * 320 + d * 2) = (u16)pk2(s, 0.f);
    }
    bar();
    const u32 T = E + 1;
    {
        f32x4 acc = {0, 0, 0, 0};
        const char* qa = smem + H_QB + (n & 3) * 320 + g * 16;
#pragma unroll
        for (int s = 0; s < 4; s++) acc = mf16(uk[s], lds128(qa + s * 64), acc);
        const float sc = 0.08838834764831845f * 1.4426950408889634f;
        const u32 tg = T & 0xffffu;
        u32x4 o;
#pragma unroll
        for (int r = 0; r < 4; r++) o[r] = (pk2(0.f, acc[r] * sc) & 0xffff0000u) | tg;
        asm volatile("s_waitcnt vmcnt(0)" ::: "memory");
        if (n < 4) stb(o, rws, (u32)(((n * 4 + cq) * 32 + h) * 512 + (16 * w + 4 * g) * 4), WS_QA);
    }
    STAMP(3);
    const u32 tg12 = T & 0xfffu;
    const int nunits = (ii < 2) ? 2 : 1;
    for (int un = 0; un < nunits; un++) {
        const int u = un == 0 ? 16 * xc + ii : 128 + 2 * xc + ii;
        const rsrc_t rg = mkrs(w < 4 ? wg : wu, 576 * 4608 * 2);
        u32x4 f0[8], f1[8];
        const u32 vg = (u32)(((n >> 2) * 32 + 8 * g) * 9216 + (n & 3) * 16 + u * 64);
        ld8<AUX_FFN>(f0, rg, vg, (w & 3) * 128 * 9216, 9216);
        {
            const rsrc_t rd = mkrs(w < 5 ? wd : (w == 5 ? wg : wu), 576 * 4608 * 2);
            const u32 vd = (w == 4 && n >= 8) ? OOB : (u32)(8 * g * 1152 + n * 16);
            const u32 vh = (n >= 8 || w == 7) ? OOB : vg;
            ld8<AUX_FFN>(f1, rd, w < 5 ? vd : vh, w < 5 ? u * 32 * 1152 + w * 256 : 512 * 9216, w < 5 ? 1152 : 9216);
        }
        pin();
        const char* xa = smem + H_XN + (n & 3) * XN_STRIDE + ((n >> 2) * 32 + 8 * g) * 2;
        float* red = (float*)(smem + H_RED);
        const bool dv = (g == (n >> 2));
        {
            f32x4 acc[8];
#pragma unroll
            for (int j = 0; j < 8; j++) acc[j] = f32x4{0, 0, 0, 0};
            gemv8(f0, lds128(xa + (w & 3) * 256), acc);
            if (dv) {
                float* p = red + (((w >> 2) * 5 + (w & 3)) * 4 + g) * 128 + (n & 3) * 8;
#pragma unroll
                for (int b = 0; b < 4; b++) {
                    *(f32x4*)(p + b * 32) = f32x4{acc[0][b], acc[1][b], acc[2][b], acc[3][b]};
                    *(f32x4*)(p + b * 32 + 4) = f32x4{acc[4][b], acc[5][b], acc[6][b], acc[7][b]};
                }
            }
        }
        if (w == 5 || w == 6) {
            f32x4 acc[8];
#pragma unroll
            for (int j = 0; j < 8; j++) acc[j] = f32x4{0, 0, 0, 0};
            u32x4 a = lds128(smem + H_XN + (n & 3) * XN_STRIDE + (512 + (((n >> 2) & 1) * 32 + 8 * g)) * 2);
            gemv8(f1, a, acc);
            if (dv) {
                float* p = red + (((w - 5) * 5 + 4) * 4 + g) * 128 + (n & 3) * 8;
#pragma unroll
                for (int b = 0; b < 4; b++) {
                    *(f32x4*)(p + b * 32) = f32x4{acc[0][b], acc[1][b], acc[2][b], acc[3][b]};
                    *(f32x4*)(p + b * 32 + 4) = f32x4{acc[4][b], acc[5][b], acc[6][b], acc[7][b]};
                }
            }
        }
        bar();
        if (tid < 128) {
            float gs = 0, us = 0;
#pragma unroll
            for (int i = 0; i < 18; i++) { gs += red[i * 128 + tid]; us += red[(20 + i) * 128 + tid]; }
            const float gg = rbf(gs), uu = rbf(us);
            const float inner = 0.7978845608028654f * (gg + 0.044715f * gg * gg * gg);
            const float ge = rbf(0.5f * gg * (1.0f + tanhf(inner)));
            *(u16*)(smem + H_HB + (tid >> 5) * 80 + (tid & 31) * 2) = (u16)pk2(ge * uu, 0.f);
        }
        bar();
        if (w < 5) {
            f32x4 acc[8];
#pragma unroll
            for (int j = 0; j < 8; j++) acc[j] = f32x4{0, 0, 0, 0};
            gemv8(f1, lds128(smem + H_HB + (n >> 2) * 80 + g * 16), acc);
            if (w < 4 || n < 8) {
                const u32 vo = (u32)(((g * 18 + 4 * w + (n >> 2)) * 144 + u) * 128 + (n & 3) * 32);
                stb(u32x4{tag12(acc[0][0], tg12), tag12(acc[1][0], tg12), tag12(acc[2][0], tg12), tag12(acc[3][0], tg12)}, rws, vo, WS_YP);
                stb(u32x4{tag12(acc[4][0], tg12), tag12(acc[5][0], tg12), tag12(acc[6][0], tg12), tag12(acc[7][0], tg12)}, rws, vo + 16, WS_YP);
            }
        }
    }
    STAMP(4);
    if (k >= 56) {
        const int r = k - 56, b = r / 18, m0 = (r - b * 18) * 32;
        float xres = 0.f;
        if (tid < 32) xres = lo2f(x[b * 576 + m0 + tid]);
        f32x4 ys = {0, 0, 0, 0}, ss = {0, 0, 0, 0};
        {
            const u32 vo = (u32)(r * 144 * 128 + tid * 16);
            for (int it = 0; it < SPIN_MAX; it++) {
                u32x4 a = ldb<AUX_SC1>(rws, vo, WS_YP), c = ldb<AUX_SC1>(rws, vo + 8192, WS_YP);
                u32x4 d = ldb<AUX_SC1>(rws, tid < 128 ? vo + 16384 : OOB, WS_YP);
                u32 bad = 0;
#pragma unroll
                for (int e = 0; e < 4; e++) bad |= (a[e] ^ tg12) | (c[e] ^ tg12) | (tid < 128 ? (d[e] ^ tg12) : 0u);
                if (__all((bad & 0xfffu) == 0) || it == SPIN_MAX - 1) {
#pragma unroll
                    for (int e = 0; e < 4; e++) ys[e] = __uint_as_float(a[e] & 0xfffff000u) + __uint_as_float(c[e] & 0xfffff000u) + __uint_as_float(d[e] & 0xfffff000u);
                    break;
                }
                __builtin_amdgcn_s_sleep(4);
            }
        }
        STAMP(5);
        {
            const u32 vo = (u32)(r * 128 * 128 + tid * 16);
            for (int it = 0; it < SPIN_MAX; it++) {
                u32x4 a = ldb<AUX_SC1>(rws, vo, WS_SP), c = ldb<AUX_SC1>(rws, vo + 8192, WS_SP);
                u32 bad = 0;
#pragma unroll
                for (int e = 0; e < 4; e++) bad |= (a[e] ^ tg12) | (c[e] ^ tg12);
                if (__all((bad & 0xfffu) == 0) || it == SPIN_MAX - 1) {
#pragma unroll
                    for (int e = 0; e < 4; e++) ss[e] = __uint_as_float(a[e] & 0xfffff000u) + __uint_as_float(c[e] & 0xfffff000u);
                    break;
                }
                __builtin_amdgcn_s_sleep(1);
            }
        }
        STAMP(6);
#pragma unroll
        for (int e = 0; e < 4; e++) {
            ys[e] += dpp<0x128>(ys[e]); ss[e] += dpp<0x128>(ss[e]);
            ys[e] = swap32sum(swap16sum(ys[e])); ss[e] = swap32sum(swap16sum(ss[e]));
        }
        float* fin = (float*)(smem + H_FIN);
        if (l < 8) { *(f32x4*)(fin + w * 32 + l * 4) = ys; *(f32x4*)(fin + 256 + w * 32 + l * 4) = ss; }
        bar();
        if (tid < 32) {
            float y = 0, s = 0;
#pragma unroll
            for (int i = 0; i < 8; i++) { y += fin[i * 32 + tid]; s += fin[256 + i * 32 + tid]; }
            y = rbf(y); s = rbf(s);
            const float gt = rbf(y / (1.0f + __expf(-y)));
            out[b * 576 + m0 + tid] = (u16)pk2(xres + rbf(s * gt), 0.f);
        }
        if (k == 56 && tid == 0) __hip_atomic_store((u32*)ws, T, __ATOMIC_RELAXED, __HIP_MEMORY_SCOPE_AGENT);
        STAMP(7);
    }
}

#define A_Q 131072
DI void dma_row(const u16* gbase, u32 voff, u32 ldsaddr) {
    u32 keep;
    asm volatile("s_mov_b32 %0, m0\n\ts_mov_b32 m0, %3\n\ts_nop 0\n\tglobal_load_lds_dwordx4 %1, %2" KV_NT "\n\ts_mov_b32 m0, %0"
                 : "=&s"(keep) : "v"(voff), "s"(gbase), "s"(ldsaddr) : "memory");
}
template <int OFF> DI void ld_sc1_asm(u32x4& r, u32 voff, const char* base) {
    asm volatile("global_load_dwordx4 %0, %1, %2 offset:%3 sc1" : "=&v"(r) : "v"(voff), "s"(base), "n"(OFF) : "memory");
}

DI void a_weights(u32x4 (&w0)[8], u32x4 (&w1)[8], u32x4 (&w2)[8], const u16* wuv, const u16* wo, int w, int h, int al, int n, int g) {
    const int kb = w & 3;
    const rsrc_t ro = mkrs(wo, 4096 * 576 * 2);
    const u32 vo = (u32)(8 * g * 1152 + n * 16);
    const u32 so = (u32)((h * 128 + 32 * kb) * 1152);
    if (w < 4) {
        const rsrc_t rv = mkrs(wuv, 512 * 4096 * 2);
        ld8<AUX_WUV>(w0, rv, (u32)(8 * g * 8192 + h * 256 + n * 16), (128 * al + 32 * kb) * 8192, 8192);
    } else {
        ld8<AUX_WO>(w0, ro, vo, so, 1152);
    }
    ld8<AUX_WO>(w1, ro, vo, so + (1 + (w >> 2)) * 256, 1152);
    ld8<AUX_WO>(w2, ro, (w >= 4 && n >= 8) ? OOB : vo, so + (3 + (w >> 2)) * 256, 1152);
}

DI void a_role(char* smem, const u16* ckv, const u16* wuv, const u16* wo, char* ws) {
    const int tid = threadIdx.x, l = tid & 63, w = __builtin_amdgcn_readfirstlane(tid >> 6);
    const int a = blockIdx.x - 128, xc = a & 7, ii = a >> 3;
    const int b = xc >> 1, chunk = (xc & 1) * 16 + ii;
    const int h = 4 * xc + (ii >> 2), al = ii & 3;
    const int n = l & 15, g = l >> 4, hh = l >> 5;
    STAMP(0);
    const rsrc_t rws = mkrs(ws, WS_END);
    u32 E;
    asm volatile("global_load_dword %0, %1, %2 sc1" : "=&v"(E) : "v"(0u), "s"(ws) : "memory");
    const u32 lds0 = (u32)(size_t)(LAS char*)smem;
    const u16* kb0 = ckv + ((size_t)b * 4096 + chunk * 128 + 16 * w) * 512;
    const u32 ldsw = lds0 + w * 16384;
#pragma unroll
    for (int i = 0; i < DMA_SPLIT; i++) dma_row(kb0, (u32)(((l ^ (((i & 3) << 2) | (i >> 2))) << 4) + i * 1024), ldsw + i * 1024);
    asm volatile("s_waitcnt vmcnt(%1)" : "+v"(E) : "n"(DMA_WAIT) : "memory");
    const u32 T = E + 1;
    {
        const char* qsrc = ws + WS_QA + b * 65536 + w * 8192;
        const u32 vq = l * 16 + 4096;
        const u32 tg = T & 0xffffu;
        u32x4 q[8];
        STAMP(1);
        bool first = true;
        for (int it = 0; it < SPIN_MAX; it++) {
            ld_sc1_asm<-4096>(q[0], vq, qsrc); ld_sc1_asm<-3072>(q[1], vq, qsrc);
            ld_sc1_asm<-2048>(q[2], vq, qsrc); ld_sc1_asm<-1024>(q[3], vq, qsrc);
            ld_sc1_asm<0>(q[4], vq, qsrc); ld_sc1_asm<1024>(q[5], vq, qsrc);
            ld_sc1_asm<2048>(q[6], vq, qsrc); ld_sc1_asm<3072>(q[7], vq, qsrc);
            if (first) {
                first = false;
#pragma unroll
                for (int i = DMA_SPLIT; i < 16; i++) dma_row(kb0, (u32)(((l ^ (((i & 3) << 2) | (i >> 2))) << 4) + i * 1024), ldsw + i * 1024);
                asm volatile("s_waitcnt vmcnt(%8)" : "+v"(q[0]), "+v"(q[1]), "+v"(q[2]), "+v"(q[3]), "+v"(q[4]), "+v"(q[5]), "+v"(q[6]), "+v"(q[7]) : "n"(16 - DMA_SPLIT) : "memory");
            } else {
                asm volatile("s_waitcnt vmcnt(0)" : "+v"(q[0]), "+v"(q[1]), "+v"(q[2]), "+v"(q[3]), "+v"(q[4]), "+v"(q[5]), "+v"(q[6]), "+v"(q[7]) :: "memory");
            }
            u32 bad = 0;
#pragma unroll
            for (int i = 0; i < 8; i++) bad |= (q[i][0] ^ tg) | (q[i][1] ^ tg) | (q[i][2] ^ tg) | (q[i][3] ^ tg);
            if (__all((bad & 0xffffu) == 0)) break;
            __builtin_amdgcn_s_sleep(1);
        }
        STAMP(2);
        const int z = (l >> 1) & 7;
        char* qd = smem + A_Q + (8 * (w >> 1) + ((l & 31) >> 2)) * 1024 + ((l >> 1) & 1) * 512 + (l & 1) * 8;
#pragma unroll
        for (int i = 0; i < 8; i++) {
            const int hq = 16 * (w & 1) + 2 * i + hh;
            u32x2 o = {__builtin_amdgcn_perm(q[i][1], q[i][0], 0x07060302u), __builtin_amdgcn_perm(q[i][3], q[i][2], 0x07060302u)};
            *(u32x2*)(qd + ((hq ^ z) << 4)) = o;
        }
    }
    u32x4 w0[8], w1[8], w2[8];
    asm volatile("s_waitcnt vmcnt(0)" ::: "memory");
    a_weights(w0, w1, w2, wuv, wo, w, h, al, n, g);
    pin();
    bar();
    STAMP(3);
    f32x16 sacc;
    float mx = 0.f;
    if (w < 4) {
#pragma unroll
        for (int r = 0; r < 16; r++) sacc[r] = 0.f;
        const int row = 32 * w + (l & 31);
        const int f = ((row & 3) << 2) | ((row >> 2) & 3);
        const char* kp = smem + row * 1024;
        const int fx = (f ^ hh) << 4;
        const char* qp = smem + A_Q + hh * 512;
#pragma unroll
        for (int s = 0; s < 32; s++) {
            const u32x4 ka = lds128(kp + ((32 * (s & 7)) ^ fx) + 256 * (s >> 3));
            const u32x4 qb = lds128(qp + s * 1024 + (((l & 31) ^ ((s & 3) * 2 + hh)) << 4));
            sacc = mf32(ka, qb, sacc);
        }
        mx = sacc[0];
#pragma unroll
        for (int r = 1; r < 16; r++) mx = fmaxf(mx, sacc[r]);
        mx = swap32max(mx);
    }
    bar();
    float* aux = (float*)(smem + A_Q);
    if (w < 4 && l < 32) aux[w * 32 + l] = mx;
    bar();
    if (w < 4) {
        const int hq = l & 31;
        const float M = fmaxf(fmaxf(aux[hq], aux[32 + hq]), fmaxf(aux[64 + hq], aux[96 + hq]));
        float p[16], ls = 0.f;
#pragma unroll
        for (int r = 0; r < 16; r++) { p[r] = __builtin_amdgcn_exp2f(sacc[r] - M); ls += p[r]; }
        ls = swap32sum(ls);
        if (l < 32) aux[128 + w * 32 + l] = ls;
        char* pd = smem + A_Q + 1024 + (2 * w) * 1024 + l * 16;
        *(u32x4*)pd = u32x4{pk2(p[0], p[1]), pk2(p[2], p[3]), pk2(p[4], p[5]), pk2(p[6], p[7])};
        *(u32x4*)(pd + 1024) = u32x4{pk2(p[8], p[9]), pk2(p[10], p[11]), pk2(p[12], p[13]), pk2(p[14], p[15])};
    }
    bar();
    STAMP(4);
    if (w == 4 && l < 32) {
        const float M = fmaxf(fmaxf(aux[l], aux[32 + l]), fmaxf(aux[64 + l], aux[96 + l]));
        const float L = (aux[128 + l] + aux[160 + l]) + (aux[192 + l] + aux[224 + l]);
        u32x2 o = {min(__float_as_uint(M), 0xfffffffeu), min(__float_as_uint(L), 0xfffffffeu)};
        __builtin_amdgcn_raw_buffer_store_b64(o, rws, (u32)(l * 1024 + (b * 32 + chunk) * 8), WS_ST, AUX_SC1);
    }
    {
        f32x16 o0, o1;
#pragma unroll
        for (int r = 0; r < 16; r++) { o0[r] = 0.f; o1[r] = 0.f; }
        const int q4 = (l & 15) >> 2, p4 = l & 3, blk = (l >> 4) & 1;
        const char* vb = smem + (4 * hh + q4) * 1024 + (w >> 1) * 256 + (p4 >> 1) * 8;
        const int cl = 8 * (w & 1) + 2 * blk + (p4 & 1);
        const char* pp = smem + A_Q + 1024 + l * 16;
#pragma unroll
        for (int ks = 0; ks < 8; ks++) {
            const u32x4 pb = lds128(pp + ks * 1024);
            u32x4 va[2];
#pragma unroll
            for (int cb = 0; cb < 2; cb++) {
#pragma unroll
                for (int t = 0; t < 2; t++) {
                    const int f = (q4 << 2) | (hh + 2 * t);
                    const char* ad = vb + ks * 16384 + t * 8192 + (((cl + 4 * cb) ^ f) << 4);
                    s16x4 v = __builtin_amdgcn_ds_read_tr16_b64_v4i16((LAS s16x4*)ad);
                    u32x2 vv = __builtin_bit_cast(u32x2, v);
                    va[cb][2 * t] = vv[0]; va[cb][2 * t + 1] = vv[1];
                }
            }
            o0 = mf32(va[0], pb, o0);
            o1 = mf32(va[1], pb, o1);
        }
        STAMP(5);
        const u32 vo = (u32)((((l & 31) * 4 + (w >> 1)) * 128 + b * 32 + chunk) * 256 + (64 * (w & 1) + 8 * hh) * 2);
#pragma unroll
        for (int i = 0; i < 2; i++) {
            u32x4 s0, s1;
#pragma unroll
            for (int e = 0; e < 4; e++) {
                s0[e] = min(pk2(o0[8 * i + 2 * e], o0[8 * i + 2 * e + 1]), 0xfffffffeu);
                s1[e] = min(pk2(o1[8 * i + 2 * e], o1[8 * i + 2 * e + 1]), 0xfffffffeu);
            }
            stb(s0, rws, vo + i * 32, WS_PART);
            stb(s1, rws, vo + 64 + i * 32, WS_PART);
        }
    }
    STAMP(6);
    u32x4 t0[8], t1[8], t2[8];
    tr8(w0, t0); tr8(w1, t1); tr8(w2, t2);
#pragma unroll
    for (int j = 0; j < 8; j++) asm volatile("" : "+v"(t0[j]), "+v"(t1[j]), "+v"(t2[j]));
    STAMP(7);
#define D_WT (A_Q + 16384)
#define D_OB 4096
#define D_CX 8192
#define D_SR 16384
    u32x4 pt[8];
    if (w < 4) {
        const u32 vo = (u32)((w * 32 + 8 * g) * 256 + n * 16);
        const u32 so = WS_PART + (h * 4 + al) * 32768;
        u32x4 pr[8];
        for (int it = 0; it < SPIN_MAX; it++) {
#pragma unroll
            for (int i = 0; i < 8; i++) pr[i] = ldb<AUX_SC1>(rws, vo + i * 256, so);
            u32 mxv = 0;
#pragma unroll
            for (int i = 0; i < 8; i++) mxv = max(max(mxv, max(pr[i][0], pr[i][1])), max(pr[i][2], pr[i][3]));
            if (__all(mxv != 0xffffffffu)) break;
            __builtin_amdgcn_s_sleep(1);
        }
        tr8(pr, pt);
    }
    u32x4 wt = {0, 0, 0, 0};
    if (w >= 4) {
        const u32 vo = (u32)(h * 1024 + ((w - 4) * 32 + 8 * g) * 8);
        u32x4 sr[4];
        for (int it = 0; it < SPIN_MAX; it++) {
#pragma unroll
            for (int i = 0; i < 4; i++) sr[i] = ldb<AUX_SC1>(rws, vo + i * 16, WS_ST);
            u32 mxv = 0;
#pragma unroll
            for (int i = 0; i < 4; i++) mxv = max(max(mxv, max(sr[i][0], sr[i][1])), max(sr[i][2], sr[i][3]));
            if (__all(mxv != 0xffffffffu)) break;
            __builtin_amdgcn_s_sleep(1);
        }
        float m[8], ll[8];
#pragma unroll
        for (int i = 0; i < 4; i++) { m[2 * i] = __uint_as_float(sr[i][0]); ll[2 * i] = __uint_as_float(sr[i][1]); m[2 * i + 1] = __uint_as_float(sr[i][2]); ll[2 * i + 1] = __uint_as_float(sr[i][3]); }
        float M = m[0];
#pragma unroll
        for (int i = 1; i < 8; i++) M = fmaxf(M, m[i]);
        M = swap32max(swap16max(M));
        float L = 0.f;
#pragma unroll
        for (int i = 0; i < 8; i++) { m[i] = __builtin_amdgcn_exp2f(m[i] - M); L += m[i] * ll[i]; }
        L = swap32sum(swap16sum(L));
        const float inv = 1.0f / L;
#pragma unroll
        for (int p = 0; p < 4; p++) {
            const float a0 = m[2 * p] * inv, a1 = m[2 * p + 1] * inv;
            const u32 hi = pk2(a0, a1);
            const u32 lo = pk2(a0 - lo2f(hi), a1 - hi2f(hi));
            wt[p] = n == 0 ? hi : (n == 1 ? lo : 0u);
        }
    }
    if (w >= 4) *(u32x4*)(smem + D_WT + (w - 4) * 1024 + l * 16) = wt;
    bar();
    STAMP(8);
    if (w < 4) {
        const u32x4 aw = lds128(smem + D_WT + w * 1024 + l * 16);
        float o[8];
#pragma unroll
        for (int j = 0; j < 8; j++) { f32x4 d = mf16(aw, pt[j], f32x4{0, 0, 0, 0}); o[j] = d[0] + d[1]; }
        if (g == 0) *(u32x4*)(smem + D_OB + w * 320 + n * 16) = u32x4{pk2(o[0], o[1]), pk2(o[2], o[3]), pk2(o[4], o[5]), pk2(o[6], o[7])};
    }
    bar();
    if (w < 4) {
        const u32x4 ao = lds128(smem + D_OB + (n >> 2) * 320 + (32 * w + 8 * g) * 2);
        f32x4 d[8];
#pragma unroll
        for (int j = 0; j < 8; j++) d[j] = mf16(ao, t0[j], f32x4{0, 0, 0, 0});
        float* cx = (float*)(smem + D_CX) + (w * 4 + g) * 128 + 8 * n;
        *(f32x4*)cx = f32x4{d[0][0], d[1][0], d[2][0], d[3][0]};
        *(f32x4*)(cx + 4) = f32x4{d[4][0], d[5][0], d[6][0], d[7][0]};
    }
    bar();
    {
        const int kb = w & 3;
        const float* cx = (const float*)(smem + D_CX) + (n >> 2) * 128 + 32 * kb + 8 * g;
        f32x4 lo = *(const f32x4*)cx, hi = *(const f32x4*)(cx + 4);
#pragma unroll
        for (int i = 1; i < 4; i++) { lo += *(const f32x4*)(cx + i * 512); hi += *(const f32x4*)(cx + i * 512 + 4); }
        const u32x4 ac = {pk2(lo[0], lo[1]), pk2(lo[2], lo[3]), pk2(hi[0], hi[1]), pk2(hi[2], hi[3])};
        float* sr = (float*)(smem + D_SR) + (kb * 4 + g) * 640 + 8 * n;
        f32x4 d[8];
        if (w >= 4) {
#pragma unroll
            for (int j = 0; j < 8; j++) d[j] = mf16(ac, t0[j], f32x4{0, 0, 0, 0});
            *(f32x4*)sr = f32x4{d[0][0], d[1][0], d[2][0], d[3][0]};
            *(f32x4*)(sr + 4) = f32x4{d[4][0], d[5][0], d[6][0], d[7][0]};
        }
#pragma unroll
        for (int j = 0; j < 8; j++) d[j] = mf16(ac, t1[j], f32x4{0, 0, 0, 0});
        float* s1 = sr + (1 + (w >> 2)) * 128;
        *(f32x4*)s1 = f32x4{d[0][0], d[1][0], d[2][0], d[3][0]};
        *(f32x4*)(s1 + 4) = f32x4{d[4][0], d[5][0], d[6][0], d[7][0]};
#pragma unroll
        for (int j = 0; j < 8; j++) d[j] = mf16(ac, t2[j], f32x4{0, 0, 0, 0});
        float* s2 = sr + (3 + (w >> 2)) * 128;
        *(f32x4*)s2 = f32x4{d[0][0], d[1][0], d[2][0], d[3][0]};
        *(f32x4*)(s2 + 4) = f32x4{d[4][0], d[5][0], d[6][0], d[7][0]};
    }
    bar();
    {
        const u32 tg12 = T & 0xfffu;
#pragma unroll
        for (int i = 0; i < 2; i++) {
            const int ch = tid + 512 * i;
            if (ch < 576) {
                const int bb = ch / 144, m = (ch - bb * 144) * 4;
                const float* sr = (const float*)(smem + D_SR) + bb * 640 + m;
                f32x4 s = *(const f32x4*)sr;
#pragma unroll
                for (int q = 1; q < 4; q++) s += *(const f32x4*)(sr + q * 2560);
                stb(u32x4{tag12(s[0], tg12), tag12(s[1], tg12), tag12(s[2], tg12), tag12(s[3], tg12)}, rws,
                    (u32)(((bb * 18 + (m >> 5)) * 128 + a) * 128 + (m & 31) * 4), WS_SP);
            }
        }
    }
    STAMP(9);
}

__global__ __launch_bounds__(512, 2) void fused_decode(const u16* x, const u16* ckv, const u16* wq, const u16* wuk, const u16* wuv,
                                                       const u16* wo, const u16* wg, const u16* wu, const u16* wd, u16* out, char* ws) {
    extern __shared__ __attribute__((aligned(16))) char smem[];
    if (blockIdx.x < 128) h_role(smem, x, wq, wuk, wg, wu, wd, out, ws);
    else a_role(smem, ckv, wuv, wo, ws);
}

extern "C" void kernel_launch(void* const* d_in, const int* in_sizes, int n_in, void* d_out, int out_size, void* d_ws, size_t ws_size,
                              hipStream_t stream) {
    (void)in_sizes; (void)n_in; (void)out_size; (void)ws_size;
    hipFuncSetAttribute((const void*)fused_decode, hipFuncAttributeMaxDynamicSharedMemorySize, 163840);
    fused_decode<<<256, 512, 163840, stream>>>((const u16*)d_in[0], (const u16*)d_in[1], (const u16*)d_in[2], (const u16*)d_in[3],
                                               (const u16*)d_in[4], (const u16*)d_in[5], (const u16*)d_in[7], (const u16*)d_in[8],
                                               (const u16*)d_in[9], (u16*)d_out, (char*)d_ws);
}
```

```cpp
#include <hip/hip_runtime.h>
typedef unsigned short u16;
typedef unsigned int u32;
typedef unsigned long long u64;
typedef u32 u32x4 __attribute__((ext_vector_type(4)));
typedef u32 u32x2 __attribute__((ext_vector_type(2)));
typedef float f32x4 __attribute__((ext_vector_type(4)));
typedef float f32x2 __attribute__((ext_vector_type(2)));
typedef float f32x16 __attribute__((ext_vector_type(16)));
typedef __bf16 bf16x8 __attribute__((ext_vector_type(8)));
typedef __bf16 bf16x2 __attribute__((ext_vector_type(2)));
typedef short s16x4 __attribute__((ext_vector_type(4)));
typedef __amdgpu_buffer_rsrc_t rsrc_t;
#define DI __device__ __forceinline__
#define LAS __attribute__((address_space(3)))

#define WS_QA   4096u
#define WS_PART (WS_QA + 262144u)
#define WS_ST   (WS_PART + 4194304u)
#define WS_SP   (WS_ST + 32768u)
#define WS_YP   (WS_SP + 1179648u)
#define WS_END  (WS_YP + 1327104u)
#define WS_DBG  (8u << 20)

#define SPIN_MAX 20000
#define AUX_WQ 0
#define AUX_WUK 2
#define AUX_FFN 2
#define AUX_WUV 2
#define AUX_WO 0
#define KV_NT " nt"
#define DMA_SPLIT 12
#define DMA_WAIT 4
#define ZPRIME 1
#define FFN_DELAY 0
#define AUX_SC1 16
#define OOB 0x80000000u

#define STAMP_DECL
#define STAMP(i) do { } while (0)
#define STAMP_FLUSH

DI float lo2f(u32 v) { return __uint_as_float(v << 16); }
DI float hi2f(u32 v) { return __uint_as_float(v & 0xffff0000u); }
DI u32 pk2(float lo, float hi) { f32x2 v = {lo, hi}; return __builtin_bit_cast(u32, __builtin_convertvector(v, bf16x2)); }
DI float rbf(float f) { return lo2f(pk2(f, 0.f)); }
DI rsrc_t mkrs(const void* p, u32 bytes) { return __builtin_amdgcn_make_buffer_rsrc((void*)p, 0, (int)bytes, 0x00020000); }
template <int AUX> DI u32x4 ldb(rsrc_t rs, u32 voff, u32 soff) { return __builtin_amdgcn_raw_buffer_load_b128(rs, voff, soff, AUX); }
DI void stb(u32x4 v, rsrc_t rs, u32 voff, u32 soff) { __builtin_amdgcn_raw_buffer_store_b128(v, rs, voff, soff, AUX_SC1); }
DI void bar() { asm volatile("s_waitcnt lgkmcnt(0)\n\ts_barrier" ::: "memory"); }
DI void pin() { asm volatile("" ::: "memory"); }
DI f32x4 mf16(u32x4 a, u32x4 b, f32x4 c) { return __builtin_amdgcn_mfma_f32_16x16x32_bf16(__builtin_bit_cast(bf16x8, a), __builtin_bit_cast(bf16x8, b), c, 0, 0, 0); }
DI f32x16 mf32(u32x4 a, u32x4 b, f32x16 c) { return __builtin_amdgcn_mfma_f32_32x32x16_bf16(__builtin_bit_cast(bf16x8, a), __builtin_bit_cast(bf16x8, b), c, 0, 0, 0); }
DI float swap32max(float v) { auto r = __builtin_amdgcn_permlane32_swap(__float_as_uint(v), __float_as_uint(v), false, false); return fmaxf(__uint_as_float(r[0]), __uint_as_float(r[1])); }
DI float swap32sum(float v) { auto r = __builtin_amdgcn_permlane32_swap(__float_as_uint(v), __float_as_uint(v), false, false); return __uint_as_float(r[0]) + __uint_as_float(r[1]); }
DI float swap16max(float v) { auto r = __builtin_amdgcn_permlane16_swap(__float_as_uint(v), __float_as_uint(v), false, false); return fmaxf(__uint_as_float(r[0]), __uint_as_float(r[1])); }
DI float swap16sum(float v) { auto r = __builtin_amdgcn_permlane16_swap(__float_as_uint(v), __float_as_uint(v), false, false); return __uint_as_float(r[0]) + __uint_as_float(r[1]); }
template <int CTRL> DI float dpp(float v) { return __uint_as_float(__builtin_amdgcn_update_dpp(0, __float_as_uint(v), CTRL, 0xf, 0xf, true)); }
DI float wave_sum(float v) {
    v += dpp<0xb1>(v);
    v += dpp<0x4e>(v);
    v += dpp<0x141>(v);
    v += dpp<0x140>(v);
    v = swap16sum(v);
    return swap32sum(v);
}
DI void tr8(const u32x4 (&r)[8], u32x4 (&t)[8]) {
#pragma unroll
    for (int jj = 0; jj < 4; jj++) {
#pragma unroll
        for (int p = 0; p < 4; p++) {
            t[2 * jj][p] = __builtin_amdgcn_perm(r[2 * p + 1][jj], r[2 * p][jj], 0x05040100u);
            t[2 * jj + 1][p] = __builtin_amdgcn_perm(r[2 * p + 1][jj], r[2 * p][jj], 0x07060302u);
        }
    }
}
template <int AUX> DI void ld8(u32x4 (&r)[8], rsrc_t rs, u32 voff, u32 soff, u32 stride) {
#pragma unroll
    for (int i = 0; i < 8; i++) r[i] = ldb<AUX>(rs, voff, soff + i * stride);
}
DI void gemv8(const u32x4 (&r)[8], u32x4 a, f32x4 (&acc)[8]) {
    u32x4 t[8]; tr8(r, t);
#pragma unroll
    for (int j = 0; j < 8; j++) acc[j] = mf16(a, t[j], acc[j]);
}
DI u32 tag12(float v, u32 tg) { return ((__float_as_uint(v) + 0x800u) & 0xfffff000u) | tg; }
DI u32x4 lds128(const char* p) { return *(const u32x4*)p; }

#define XN_STRIDE 1216
#define H_XN   0
#define H_RED  5120
#define H_QB   (H_RED + 20480)
#define H_HB   (H_QB + 1280)
#define H_FIN  (H_HB + 512)

DI void h_role(char* smem, const u16* x, const u16* wq, const u16* wuk, const u16* wg, const u16* wu, const u16* wd,
               u16* out, char* ws) {
    const int tid = threadIdx.x, l = tid & 63, w = __builtin_amdgcn_readfirstlane(tid >> 6);
    const int k = blockIdx.x, xc = k & 7, ii = k >> 3, h = 4 * xc + (ii >> 2), cq = ii & 3;
    const int n = l & 15, g = l >> 4;
    STAMP_DECL;
    STAMP(0);
    const rsrc_t rws = mkrs(ws, WS_END);
    const u32 E = __hip_atomic_load((const u32*)ws, __ATOMIC_RELAXED, __HIP_MEMORY_SCOPE_AGENT);
    u32x2 xv[3];
    {
        const rsrc_t rx = mkrs(x, 4 * 576 * 2);
        const u32 vo = (w < 4) ? (u32)(w * 1152 + l * 8) : OOB;
        xv[0] = __builtin_amdgcn_raw_buffer_load_b64(rx, vo, 0, 0);
        xv[1] = __builtin_amdgcn_raw_buffer_load_b64(rx, vo, 512, 0);
        xv[2] = __builtin_amdgcn_raw_buffer_load_b64(rx, l < 16 ? vo : OOB, 1024, 0);
    }
    const rsrc_t rq = mkrs(wq, 576 * 4096 * 2);
    const int rot = (9 * cq) >> 1;
    int b0 = w + rot; if (b0 >= 18) b0 -= 18;
    int b1 = w + 8 + rot; if (b1 >= 18) b1 -= 18;
    int b2 = w + 16 + rot; if (b2 >= 18) b2 -= 18;
    const u32 vq = (u32)(8 * g * 8192 + h * 256 + n * 16);
    u32x4 r0[8], r1[8], r2[8];
    ld8<AUX_WQ>(r0, rq, vq, b0 * 262144, 8192);
    ld8<AUX_WQ>(r1, rq, vq, b1 * 262144, 8192);
    ld8<AUX_WQ>(r2, rq, w < 2 ? vq : OOB, b2 * 262144, 8192);
    u32x4 uk[4];
    {
        const rsrc_t ru = mkrs(wuk, 512 * 4096 * 2);
        const u32 vo = (u32)(((128 * cq + 16 * w + n) * 32 + h) * 256 + g * 16);
#pragma unroll
        for (int s = 0; s < 4; s++) uk[s] = ldb<AUX_WUK>(ru, vo + s * 64, 0);
    }
    {
        const u32x4 ones = {~0u, ~0u, ~0u, ~0u};
#pragma unroll
        for (int i = 0; i < 4; i++) stb(ones, rws, (u32)(tid * 16 + i * 8192), WS_PART + (h * 4 + cq) * 32768);
        if (tid < 16) stb(ones, rws, (u32)(tid * 16), WS_ST + h * 1024 + cq * 256);
    }
    pin();
    STAMP(8);
    if (w < 4) {
        float v[12];
#pragma unroll
        for (int i = 0; i < 3; i++) { v[4 * i] = lo2f(xv[i][0]); v[4 * i + 1] = hi2f(xv[i][0]); v[4 * i + 2] = lo2f(xv[i][1]); v[4 * i + 3] = hi2f(xv[i][1]); }
        float s = 0;
#pragma unroll
        for (int i = 0; i < 12; i++) s += v[i];
        const float mf = wave_sum(s) * (1.0f / 576);
        const float m = rbf(mf);
        float q = 0;
#pragma unroll
        for (int i = 0; i < 8; i++) { float d = v[i] - mf; q += d * d; }
        if (l < 16) {
#pragma unroll
            for (int i = 8; i < 12; i++) { float d = v[i] - mf; q += d * d; }
        }
        const float var = rbf(wave_sum(q) * (1.0f / 576));
        const float den = rbf(sqrtf(rbf(var + 1e-5f)));
#pragma unroll
        for (int i = 0; i < 3; i++) {
            u32x2 o;
            o[0] = pk2(rbf(v[4 * i] - m) / den, rbf(v[4 * i + 1] - m) / den);
            o[1] = pk2(rbf(v[4 * i + 2] - m) / den, rbf(v[4 * i + 3] - m) / den);
            if (i < 2 || l < 16) *(u32x2*)(smem + H_XN + w * XN_STRIDE + i * 512 + l * 8) = o;
        }
    }
    bar();
    STAMP(1);
    {
        f32x4 acc[8];
#pragma unroll
        for (int j = 0; j < 8; j++) acc[j] = f32x4{0, 0, 0, 0};
        const char* xa = smem + H_XN + (n >> 2) * XN_STRIDE + g * 16;
        gemv8(r0, lds128(xa + b0 * 64), acc);
        gemv8(r1, lds128(xa + b1 * 64), acc);
        if (w < 2) gemv8(r2, lds128(xa + b2 * 64), acc);
        float* red = (float*)(smem + H_RED) + (w * 4 + g) * 128 + 4 * n;
        *(f32x4*)red = f32x4{acc[0][0], acc[1][0], acc[2][0], acc[3][0]};
        *(f32x4*)(red + 64) = f32x4{acc[4][0], acc[5][0], acc[6][0], acc[7][0]};
    }
    STAMP(2);
    bar();
    {
        const float* red = (const float*)(smem + H_RED) + tid;
        float s = 0;
#pragma unroll
        for (int i = 0; i < 8; i++) s += red[i * 512];
        const int b = tid >> 7, idx = tid & 127, d = 8 * ((idx >> 2) & 15) + 4 * (idx >> 6) + (idx & 3);
        *(u16*)(smem + H_QB + b * 320 + d * 2) = (u16)pk2(s, 0.f);
    }
    bar();
    const u32 T = E + 1;
    {
        f32x4 acc = {0, 0, 0, 0};
        const char* qa = smem + H_QB + (n & 3) * 320 + g * 16;
#pragma unroll
        for (int s = 0; s < 4; s++) acc = mf16(uk[s], lds128(qa + s * 64), acc);
        const float sc = 0.08838834764831845f * 1.4426950408889634f;
        const u32 tg = T & 0xffffu;
        u32x4 o;
#pragma unroll
        for (int r = 0; r < 4; r++) o[r] = (pk2(0.f, acc[r] * sc) & 0xffff0000u) | tg;
        asm volatile("s_waitcnt vmcnt(0)" ::: "memory");
        if (n < 4) stb(o, rws, (u32)(((n * 4 + cq) * 32 + h) * 512 + (16 * w + 4 * g) * 4), WS_QA);
    }
    STAMP(3);
    const u32 tg12 = T & 0xfffu;
    const int nunits = (ii < 2) ? 2 : 1;
    for (int un = 0; un < nunits; un++) {
        const int u = un == 0 ? 16 * xc + ii : 128 + 2 * xc + ii;
        const rsrc_t rg = mkrs(w < 4 ? wg : wu, 576 * 4608 * 2);
        u32x4 f0[8], f1[8];
        const u32 vg = (u32)(((n >> 2) * 32 + 8 * g) * 9216 + (n & 3) * 16 + u * 64);
        ld8<AUX_FFN>(f0, rg, vg, (w & 3) * 128 * 9216, 9216);
        {
            const rsrc_t rd = mkrs(w < 5 ? wd : (w == 5 ? wg : wu), 576 * 4608 * 2);
            const u32 vd = (w == 4 && n >= 8) ? OOB : (u32)(8 * g * 1152 + n * 16);
            const u32 vh = (n >= 8 || w == 7) ? OOB : vg;
            ld8<AUX_FFN>(f1, rd, w < 5 ? vd : vh, w < 5 ? u * 32 * 1152 + w * 256 : 512 * 9216, w < 5 ? 1152 : 9216);
        }
        pin();
        const char* xa = smem + H_XN + (n & 3) * XN_STRIDE + ((n >> 2) * 32 + 8 * g) * 2;
        float* red = (float*)(smem + H_RED);
        const bool dv = (g == (n >> 2));
        {
            f32x4 acc[8];
#pragma unroll
            for (int j = 0; j < 8; j++) acc[j] = f32x4{0, 0, 0, 0};
            gemv8(f0, lds128(xa + (w & 3) * 256), acc);
            if (dv) {
                float* p = red + (((w >> 2) * 5 + (w & 3)) * 4 + g) * 128 + (n & 3) * 8;
#pragma unroll
                for (int b = 0; b < 4; b++) {
                    *(f32x4*)(p + b * 32) = f32x4{acc[0][b], acc[1][b], acc[2][b], acc[3][b]};
                    *(f32x4*)(p + b * 32 + 4) = f32x4{acc[4][b], acc[5][b], acc[6][b], acc[7][b]};
                }
            }
        }
        if (w == 5 || w == 6) {
            f32x4 acc[8];
#pragma unroll
            for (int j = 0; j < 8; j++) acc[j] = f32x4{0, 0, 0, 0};
            u32x4 a = lds128(smem + H_XN + (n & 3) * XN_STRIDE + (512 + (((n >> 2) & 1) * 32 + 8 * g)) * 2);
            gemv8(f1, a, acc);
            if (dv) {
                float* p = red + (((w - 5) * 5 + 4) * 4 + g) * 128 + (n & 3) * 8;
#pragma unroll
                for (int b = 0; b < 4; b++) {
                    *(f32x4*)(p + b * 32) = f32x4{acc[0][b], acc[1][b], acc[2][b], acc[3][b]};
                    *(f32x4*)(p + b * 32 + 4) = f32x4{acc[4][b], acc[5][b], acc[6][b], acc[7][b]};
                }
            }
        }
        bar();
        if (tid < 128) {
            float gs = 0, us = 0;
#pragma unroll
            for (int i = 0; i < 18; i++) { gs += red[i * 128 + tid]; us += red[(20 + i) * 128 + tid]; }
            const float gg = rbf(gs), uu = rbf(us);
            const float inner = 0.7978845608028654f * (gg + 0.044715f * gg * gg * gg);
            const float ge = rbf(0.5f * gg * (1.0f + tanhf(inner)));
            *(u16*)(smem + H_HB + (tid >> 5) * 80 + (tid & 31) * 2) = (u16)pk2(ge * uu, 0.f);
        }
        bar();
        if (w < 5) {
            f32x4 acc[8];
#pragma unroll
            for (int j = 0; j < 8; j++) acc[j] = f32x4{0, 0, 0, 0};
            gemv8(f1, lds128(smem + H_HB + (n >> 2) * 80 + g * 16), acc);
            if (w < 4 || n < 8) {
                const u32 vo = (u32)(((g * 18 + 4 * w + (n >> 2)) * 144 + u) * 128 + (n & 3) * 32);
                stb(u32x4{tag12(acc[0][0], tg12), tag12(acc[1][0], tg12), tag12(acc[2][0], tg12), tag12(acc[3][0], tg12)}, rws, vo, WS_YP);
                stb(u32x4{tag12(acc[4][0], tg12), tag12(acc[5][0], tg12), tag12(acc[6][0], tg12), tag12(acc[7][0], tg12)}, rws, vo + 16, WS_YP);
            }
        }
    }
    STAMP(4);
    if (k >= 56) {
        const int r = k - 56, b = r / 18, m0 = (r - b * 18) * 32;
        float xres = 0.f;
        if (tid < 32) xres = lo2f(x[b * 576 + m0 + tid]);
        f32x4 ys = {0, 0, 0, 0}, ss = {0, 0, 0, 0};
        {
            const u32 vo = (u32)(r * 144 * 128 + tid * 16);
            for (int it = 0; it < SPIN_MAX; it++) {
                u32x4 a = ldb<AUX_SC1>(rws, vo, WS_YP), c = ldb<AUX_SC1>(rws, vo + 8192, WS_YP);
                u32x4 d = ldb<AUX_SC1>(rws, tid < 128 ? vo + 16384 : OOB, WS_YP);
                u32 bad = 0;
#pragma unroll
                for (int e = 0; e < 4; e++) bad |= (a[e] ^ tg12) | (c[e] ^ tg12) | (tid < 128 ? (d[e] ^ tg12) : 0u);
                if (__all((bad & 0xfffu) == 0) || it == SPIN_MAX - 1) {
#pragma unroll
                    for (int e = 0; e < 4; e++) ys[e] = __uint_as_float(a[e] & 0xfffff000u) + __uint_as_float(c[e] & 0xfffff000u) + __uint_as_float(d[e] & 0xfffff000u);
                    break;
                }
                __builtin_amdgcn_s_sleep(4);
            }
        }
        STAMP(5);
        {
            const u32 vo = (u32)(r * 128 * 128 + tid * 16);
            for (int it = 0; it < SPIN_MAX; it++) {
                u32x4 a = ldb<AUX_SC1>(rws, vo, WS_SP), c = ldb<AUX_SC1>(rws, vo + 8192, WS_SP);
                u32 bad = 0;
#pragma unroll
                for (int e = 0; e < 4; e++) bad |= (a[e] ^ tg12) | (c[e] ^ tg12);
                if (__all((bad & 0xfffu) == 0) || it == SPIN_MAX - 1) {
#pragma unroll
                    for (int e = 0; e < 4; e++) ss[e] = __uint_as_float(a[e] & 0xfffff000u) + __uint_as_float(c[e] & 0xfffff000u);
                    break;
                }
                __builtin_amdgcn_s_sleep(1);
            }
        }
        STAMP(6);
#pragma unroll
        for (int e = 0; e < 4; e++) {
            ys[e] += dpp<0x128>(ys[e]); ss[e] += dpp<0x128>(ss[e]);
            ys[e] = swap32sum(swap16sum(ys[e])); ss[e] = swap32sum(swap16sum(ss[e]));
        }
        float* fin = (float*)(smem + H_FIN);
        if (l < 8) { *(f32x4*)(fin + w * 32 + l * 4) = ys; *(f32x4*)(fin + 256 + w * 32 + l * 4) = ss; }
        bar();
        if (tid < 32) {
            float y = 0, s = 0;
#pragma unroll
            for (int i = 0; i < 8; i++) { y += fin[i * 32 + tid]; s += fin[256 + i * 32 + tid]; }
            y = rbf(y); s = rbf(s);
            const float gt = rbf(y / (1.0f + __expf(-y)));
            out[b * 576 + m0 + tid] = (u16)pk2(xres + rbf(s * gt), 0.f);
        }
        if (k == 56 && tid == 0) __hip_atomic_store((u32*)ws, T, __ATOMIC_RELAXED, __HIP_MEMORY_SCOPE_AGENT);
        STAMP(7);
    }
    STAMP_FLUSH;
}

#define A_Q 131072
DI void dma_row(const u16* gbase, u32 voff, u32 ldsaddr) {
    u32 keep;
    asm volatile("s_mov_b32 %0, m0\n\ts_mov_b32 m0, %3\n\ts_nop 0\n\tglobal_load_lds_dwordx4 %1, %2" KV_NT "\n\ts_mov_b32 m0, %0"
                 : "=&s"(keep) : "v"(voff), "s"(gbase), "s"(ldsaddr) : "memory");
}
template <int OFF> DI void ld_sc1_asm(u32x4& r, u32 voff, const char* base) {
    asm volatile("global_load_dwordx4 %0, %1, %2 offset:%3 sc1" : "=&v"(r) : "v"(voff), "s"(base), "n"(OFF) : "memory");
}

DI void a_weights(u32x4 (&w0)[8], u32x4 (&w1)[8], u32x4 (&w2)[8], const u16* wuv, const u16* wo, int w, int h, int al, int n, int g) {
    const int kb = w & 3;
    const rsrc_t ro = mkrs(wo, 4096 * 576 * 2);
    const u32 vo = (u32)(8 * g * 1152 + n * 16);
    const u32 so = (u32)((h * 128 + 32 * kb) * 1152);
    if (w < 4) {
        const rsrc_t rv = mkrs(wuv, 512 * 4096 * 2);
        ld8<AUX_WUV>(w0, rv, (u32)(8 * g * 8192 + h * 256 + n * 16), (128 * al + 32 * kb) * 8192, 8192);
    } else {
        ld8<AUX_WO>(w0, ro, vo, so, 1152);
    }
    ld8<AUX_WO>(w1, ro, vo, so + (1 + (w >> 2)) * 256, 1152);
    ld8<AUX_WO>(w2, ro, (w >= 4 && n >= 8) ? OOB : vo, so + (3 + (w >> 2)) * 256, 1152);
}

DI void a_role(char* smem, const u16* ckv, const u16* wuv, const u16* wo, char* ws) {
    const int tid = threadIdx.x, l = tid & 63, w = __builtin_amdgcn_readfirstlane(tid >> 6);
    const int a = blockIdx.x - 128, xc = a & 7, ii = a >> 3;
    const int b = xc >> 1, chunk = (xc & 1) * 16 + ii;
    const int h = 4 * xc + (ii >> 2), al = ii & 3;
    const int n = l & 15, g = l >> 4, hh = l >> 5;
    STAMP_DECL;
    STAMP(0);
    const rsrc_t rws = mkrs(ws, WS_END);
    u32 E;
    asm volatile("global_load_dword %0, %1, %2 sc1" : "=&v"(E) : "v"(0u), "s"(ws) : "memory");
    const u32 lds0 = (u32)(size_t)(LAS char*)smem;
    const u16* kb0 = ckv + ((size_t)b * 4096 + chunk * 128 + 16 * w) * 512;
    const u32 ldsw = lds0 + w * 16384;
#pragma unroll
    for (int i = 0; i < DMA_SPLIT; i++) dma_row(kb0, (u32)(((l ^ (((i & 3) << 2) | (i >> 2))) << 4) + i * 1024), ldsw + i * 1024);
    asm volatile("s_waitcnt vmcnt(%1)" : "+v"(E) : "n"(DMA_WAIT) : "memory");
    const u32 T = E + 1;
    {
        const char* qsrc = ws + WS_QA + b * 65536 + w * 8192;
        const u32 vq = l * 16 + 4096;
        const u32 tg = T & 0xffffu;
        u32x4 q[8];
        STAMP(1);
        bool first = true;
        for (int it = 0; it < SPIN_MAX; it++) {
            ld_sc1_asm<-4096>(q[0], vq, qsrc); ld_sc1_asm<-3072>(q[1], vq, qsrc);
            ld_sc1_asm<-2048>(q[2], vq, qsrc); ld_sc1_asm<-1024>(q[3], vq, qsrc);
            ld_sc1_asm<0>(q[4], vq, qsrc); ld_sc1_asm<1024>(q[5], vq, qsrc);
            ld_sc1_asm<2048>(q[6], vq, qsrc); ld_sc1_asm<3072>(q[7], vq, qsrc);
            if (first) {
                first = false;
#pragma unroll
                for (int i = DMA_SPLIT; i < 16; i++) dma_row(kb0, (u32)(((l ^ (((i & 3) << 2) | (i >> 2))) << 4) + i * 1024), ldsw + i * 1024);
                asm volatile("s_waitcnt vmcnt(%8)" : "+v"(q[0]), "+v"(q[1]), "+v"(q[2]), "+v"(q[3]), "+v"(q[4]), "+v"(q[5]), "+v"(q[6]), "+v"(q[7]) : "n"(16 - DMA_SPLIT) : "memory");
            } else {
                asm volatile("s_waitcnt vmcnt(0)" : "+v"(q[0]), "+v"(q[1]), "+v"(q[2]), "+v"(q[3]), "+v"(q[4]), "+v"(q[5]), "+v"(q[6]), "+v"(q[7]) :: "memory");
            }
            u32 bad = 0;
#pragma unroll
            for (int i = 0; i < 8; i++) bad |= (q[i][0] ^ tg) | (q[i][1] ^ tg) | (q[i][2] ^ tg) | (q[i][3] ^ tg);
            if (__all((bad & 0xffffu) == 0)) break;
            __builtin_amdgcn_s_sleep(1);
        }
        STAMP(2);
        const int z = (l >> 1) & 7;
        char* qd = smem + A_Q + (8 * (w >> 1) + ((l & 31) >> 2)) * 1024 + ((l >> 1) & 1) * 512 + (l & 1) * 8;
#pragma unroll
        for (int i = 0; i < 8; i++) {
            const int hq = 16 * (w & 1) + 2 * i + hh;
            u32x2 o = {__builtin_amdgcn_perm(q[i][1], q[i][0], 0x07060302u), __builtin_amdgcn_perm(q[i][3], q[i][2], 0x07060302u)};
            *(u32x2*)(qd + ((hq ^ z) << 4)) = o;
        }
    }
    u32x4 w0[8], w1[8], w2[8];
    asm volatile("s_waitcnt vmcnt(0)" ::: "memory");
    a_weights(w0, w1, w2, wuv, wo, w, h, al, n, g);
    pin();
    bar();
    STAMP(3);
    f32x16 sacc;
    float mx = 0.f;
    if (w < 4) {
#pragma unroll
        for (int r = 0; r < 16; r++) sacc[r] = 0.f;
        const int row = 32 * w + (l & 31);
        const int f = ((row & 3) << 2) | ((row >> 2) & 3);
        const char* kp = smem + row * 1024;
        const int fx = (f ^ hh) << 4;
        const char* qp = smem + A_Q + hh * 512;
#pragma unroll
        for (int s = 0; s < 32; s++) {
            const u32x4 ka = lds128(kp + ((32 * (s & 7)) ^ fx) + 256 * (s >> 3));
            const u32x4 qb = lds128(qp + s * 1024 + (((l & 31) ^ ((s & 3) * 2 + hh)) << 4));
            sacc = mf32(ka, qb, sacc);
        }
        mx = sacc[0];
#pragma unroll
        for (int r = 1; r < 16; r++) mx = fmaxf(mx, sacc[r]);
        mx = swap32max(mx);
    }
    bar();
    float* aux = (float*)(smem + A_Q);
    if (w < 4 && l < 32) aux[w * 32 + l] = mx;
    bar();
    if (w < 4) {
        const int hq = l & 31;
        const float M = fmaxf(fmaxf(aux[hq], aux[32 + hq]), fmaxf(aux[64 + hq], aux[96 + hq]));
        float p[16], ls = 0.f;
#pragma unroll
        for (int r = 0; r < 16; r++) { p[r] = __builtin_amdgcn_exp2f(sacc[r] - M); ls += p[r]; }
        ls = swap32sum(ls);
        if (l < 32) aux[128 + w * 32 + l] = ls;
        char* pd = smem + A_Q + 1024 + (2 * w) * 1024 + l * 16;
        *(u32x4*)pd = u32x4{pk2(p[0], p[1]), pk2(p[2], p[3]), pk2(p[4], p[5]), pk2(p[6], p[7])};
        *(u32x4*)(pd + 1024) = u32x4{pk2(p[8], p[9]), pk2(p[10], p[11]), pk2(p[12], p[13]), pk2(p[14], p[15])};
    }
    bar();
    STAMP(4);
    if (w == 4 && l < 32) {
        const float M = fmaxf(fmaxf(aux[l], aux[32 + l]), fmaxf(aux[64 + l], aux[96 + l]));
        const float L = (aux[128 + l] + aux[160 + l]) + (aux[192 + l] + aux[224 + l]);
        u32x2 o = {min(__float_as_uint(M), 0xfffffffeu), min(__float_as_uint(L), 0xfffffffeu)};
        __builtin_amdgcn_raw_buffer_store_b64(o, rws, (u32)(l * 1024 + (b * 32 + chunk) * 8), WS_ST, AUX_SC1);
    }
    {
        f32x16 o0, o1;
#pragma unroll
        for (int r = 0; r < 16; r++) { o0[r] = 0.f; o1[r] = 0.f; }
        const int q4 = (l & 15) >> 2, p4 = l & 3, blk = (l >> 4) & 1;
        const char* vb = smem + (4 * hh + q4) * 1024 + (w >> 1) * 256 + (p4 >> 1) * 8;
        const int cl = 8 * (w & 1) + 2 * blk + (p4 & 1);
        const char* pp = smem + A_Q + 1024 + l * 16;
#pragma unroll
        for (int ks = 0; ks < 8; ks++) {
            const u32x4 pb = lds128(pp + ks * 1024);
            u32x4 va[2];
#pragma unroll
            for (int cb = 0; cb < 2; cb++) {
#pragma unroll
                for (int t = 0; t < 2; t++) {
                    const int f = (q4 << 2) | (hh + 2 * t);
                    const char* ad = vb + ks * 16384 + t * 8192 + (((cl + 4 * cb) ^ f) << 4);
                    s16x4 v = __builtin_amdgcn_ds_read_tr16_b64_v4i16((LAS s16x4*)ad);
                    u32x2 vv = __builtin_bit_cast(u32x2, v);
                    va[cb][2 * t] = vv[0]; va[cb][2 * t + 1] = vv[1];
                }
            }
            o0 = mf32(va[0], pb, o0);
            o1 = mf32(va[1], pb, o1);
        }
        STAMP(5);
        const u32 vo = (u32)((((l & 31) * 4 + (w >> 1)) * 128 + b * 32 + chunk) * 256 + (64 * (w & 1) + 8 * hh) * 2);
#pragma unroll
        for (int i = 0; i < 2; i++) {
            u32x4 s0, s1;
#pragma unroll
            for (int e = 0; e < 4; e++) {
                s0[e] = min(pk2(o0[8 * i + 2 * e], o0[8 * i + 2 * e + 1]), 0xfffffffeu);
                s1[e] = min(pk2(o1[8 * i + 2 * e], o1[8 * i + 2 * e + 1]), 0xfffffffeu);
            }
            stb(s0, rws, vo + i * 32, WS_PART);
            stb(s1, rws, vo + 64 + i * 32, WS_PART);
        }
    }
    STAMP(6);
    u32x4 t0[8], t1[8], t2[8];
    tr8(w0, t0); tr8(w1, t1); tr8(w2, t2);
#pragma unroll
    for (int j = 0; j < 8; j++) asm volatile("" : "+v"(t0[j]), "+v"(t1[j]), "+v"(t2[j]));
    STAMP(7);
#define D_WT (A_Q + 16384)
#define D_OB 4096
#define D_CX 8192
#define D_SR 16384
    u32x4 pt[8];
    if (w < 4) {
        const u32 vo = (u32)((w * 32 + 8 * g) * 256 + n * 16);
        const u32 so = WS_PART + (h * 4 + al) * 32768;
        u32x4 pr[8];
        for (int it = 0; it < SPIN_MAX; it++) {
#pragma unroll
            for (int i = 0; i < 8; i++) pr[i] = ldb<AUX_SC1>(rws, vo + i * 256, so);
            u32 mxv = 0;
#pragma unroll
            for (int i = 0; i < 8; i++) mxv = max(max(mxv, max(pr[i][0], pr[i][1])), max(pr[i][2], pr[i][3]));
            if (__all(mxv != 0xffffffffu)) break;
            __builtin_amdgcn_s_sleep(1);
        }
        tr8(pr, pt);
    }
    u32x4 wt = {0, 0, 0, 0};
    if (w >= 4) {
        const u32 vo = (u32)(h * 1024 + ((w - 4) * 32 + 8 * g) * 8);
        u32x4 sr[4];
        for (int it = 0; it < SPIN_MAX; it++) {
#pragma unroll
            for (int i = 0; i < 4; i++) sr[i] = ldb<AUX_SC1>(rws, vo + i * 16, WS_ST);
            u32 mxv = 0;
#pragma unroll
            for (int i = 0; i < 4; i++) mxv = max(max(mxv, max(sr[i][0], sr[i][1])), max(sr[i][2], sr[i][3]));
            if (__all(mxv != 0xffffffffu)) break;
            __builtin_amdgcn_s_sleep(1);
        }
        float m[8], ll[8];
#pragma unroll
        for (int i = 0; i < 4; i++) { m[2 * i] = __uint_as_float(sr[i][0]); ll[2 * i] = __uint_as_float(sr[i][1]); m[2 * i + 1] = __uint_as_float(sr[i][2]); ll[2 * i + 1] = __uint_as_float(sr[i][3]); }
        float M = m[0];
#pragma unroll
        for (int i = 1; i < 8; i++) M = fmaxf(M, m[i]);
        M = swap32max(swap16max(M));
        float L = 0.f;
#pragma unroll
        for (int i = 0; i < 8; i++) { m[i] = __builtin_amdgcn_exp2f(m[i] - M); L += m[i] * ll[i]; }
        L = swap32sum(swap16sum(L));
        const float inv = 1.0f / L;
#pragma unroll
        for (int p = 0; p < 4; p++) {
            const float a0 = m[2 * p] * inv, a1 = m[2 * p + 1] * inv;
            const u32 hi = pk2(a0, a1);
            const u32 lo = pk2(a0 - lo2f(hi), a1 - hi2f(hi));
            wt[p] = n == 0 ? hi : (n == 1 ? lo : 0u);
        }
    }
    if (w >= 4) *(u32x4*)(smem + D_WT + (w - 4) * 1024 + l * 16) = wt;
    bar();
    STAMP(8);
    if (w < 4) {
        const u32x4 aw = lds128(smem + D_WT + w * 1024 + l * 16);
        float o[8];
#pragma unroll
        for (int j = 0; j < 8; j++) { f32x4 d = mf16(aw, pt[j], f32x4{0, 0, 0, 0}); o[j] = d[0] + d[1]; }
        if (g == 0) *(u32x4*)(smem + D_OB + w * 320 + n * 16) = u32x4{pk2(o[0], o[1]), pk2(o[2], o[3]), pk2(o[4], o[5]), pk2(o[6], o[7])};
    }
    bar();
    if (w < 4) {
        const u32x4 ao = lds128(smem + D_OB + (n >> 2) * 320 + (32 * w + 8 * g) * 2);
        f32x4 d[8];
#pragma unroll
        for (int j = 0; j < 8; j++) d[j] = mf16(ao, t0[j], f32x4{0, 0, 0, 0});
        float* cx = (float*)(smem + D_CX) + (w * 4 + g) * 128 + 8 * n;
        *(f32x4*)cx = f32x4{d[0][0], d[1][0], d[2][0], d[3][0]};
        *(f32x4*)(cx + 4) = f32x4{d[4][0], d[5][0], d[6][0], d[7][0]};
    }
    bar();
    {
        const int kb = w & 3;
        const float* cx = (const float*)(smem + D_CX) + (n >> 2) * 128 + 32 * kb + 8 * g;
        f32x4 lo = *(const f32x4*)cx, hi = *(const f32x4*)(cx + 4);
#pragma unroll
        for (int i = 1; i < 4; i++) { lo += *(const f32x4*)(cx + i * 512); hi += *(const f32x4*)(cx + i * 512 + 4); }
        const u32x4 ac = {pk2(lo[0], lo[1]), pk2(lo[2], lo[3]), pk2(hi[0], hi[1]), pk2(hi[2], hi[3])};
        float* sr = (float*)(smem + D_SR) + (kb * 4 + g) * 640 + 8 * n;
        f32x4 d[8];
        if (w >= 4) {
#pragma unroll
            for (int j = 0; j < 8; j++) d[j] = mf16(ac, t0[j], f32x4{0, 0, 0, 0});
            *(f32x4*)sr = f32x4{d[0][0], d[1][0], d[2][0], d[3][0]};
            *(f32x4*)(sr + 4) = f32x4{d[4][0], d[5][0], d[6][0], d[7][0]};
        }
#pragma unroll
        for (int j = 0; j < 8; j++) d[j] = mf16(ac, t1[j], f32x4{0, 0, 0, 0});
        float* s1 = sr + (1 + (w >> 2)) * 128;
        *(f32x4*)s1 = f32x4{d[0][0], d[1][0], d[2][0], d[3][0]};
        *(f32x4*)(s1 + 4) = f32x4{d[4][0], d[5][0], d[6][0], d[7][0]};
#pragma unroll
        for (int j = 0; j < 8; j++) d[j] = mf16(ac, t2[j], f32x4{0, 0, 0, 0});
        float* s2 = sr + (3 + (w >> 2)) * 128;
        *(f32x4*)s2 = f32x4{d[0][0], d[1][0], d[2][0], d[3][0]};
        *(f32x4*)(s2 + 4) = f32x4{d[4][0], d[5][0], d[6][0], d[7][0]};
    }
    bar();
    {
        const u32 tg12 = T & 0xfffu;
#pragma unroll
        for (int i = 0; i < 2; i++) {
            const int ch = tid + 512 * i;
            if (ch < 576) {
                const int bb = ch / 144, m = (ch - bb * 144) * 4;
                const float* sr = (const float*)(smem + D_SR) + bb * 640 + m;
                f32x4 s = *(const f32x4*)sr;
#pragma unroll
                for (int q = 1; q < 4; q++) s += *(const f32x4*)(sr + q * 2560);
                stb(u32x4{tag12(s[0], tg12), tag12(s[1], tg12), tag12(s[2], tg12), tag12(s[3], tg12)}, rws,
                    (u32)(((bb * 18 + (m >> 5)) * 128 + a) * 128 + (m & 31) * 4), WS_SP);
            }
        }
    }
    STAMP(9);
    STAMP_FLUSH;
}

__global__ __launch_bounds__(512, 2) void fused_decode(const u16* x, const u16* ckv, const u16* wq, const u16* wuk, const u16* wuv,
                                                       const u16* wo, const u16* wg, const u16* wu, const u16* wd, u16* out, char* ws) {
    extern __shared__ __attribute__((aligned(16))) char smem[];
    if (blockIdx.x < 128) h_role(smem, x, wq, wuk, wg, wu, wd, out, ws);
    else a_role(smem, ckv, wuv, wo, ws);
}

extern "C" void kernel_launch(void* const* d_in, const int* in_sizes, int n_in, void* d_out, int out_size, void* d_ws, size_t ws_size,
                              hipStream_t stream) {
    (void)in_sizes; (void)n_in; (void)out_size; (void)ws_size;
    hipFuncSetAttribute((const void*)fused_decode, hipFuncAttributeMaxDynamicSharedMemorySize, 163840);
    fused_decode<<<256, 512, 163840, stream>>>((const u16*)d_in[0], (const u16*)d_in[1], (const u16*)d_in[2], (const u16*)d_in[3],
                                               (const u16*)d_in[4], (const u16*)d_in[5], (const u16*)d_in[7], (const u16*)d_in[8],
                                               (const u16*)d_in[9], (u16*)d_out, (char*)d_ws);
}
```

```cpp
#include <hip/hip_runtime.h>
#include <stdio.h>
#define STAMPS
#define STAMPS_PRINT
typedef unsigned short u16;
typedef unsigned int u32;
typedef unsigned long long u64;
typedef u32 u32x4 __attribute__((ext_vector_type(4)));
typedef u32 u32x2 __attribute__((ext_vector_type(2)));
typedef float f32x4 __attribute__((ext_vector_type(4)));
typedef float f32x2 __attribute__((ext_vector_type(2)));
typedef float f32x16 __attribute__((ext_vector_type(16)));
typedef __bf16 bf16x8 __attribute__((ext_vector_type(8)));
typedef __bf16 bf16x2 __attribute__((ext_vector_type(2)));
typedef short s16x4 __attribute__((ext_vector_type(4)));
typedef __amdgpu_buffer_rsrc_t rsrc_t;
#define DI __device__ __forceinline__
#define LAS __attribute__((address_space(3)))

#define WS_QA   4096u
#define WS_PART (WS_QA + 262144u)
#define WS_ST   (WS_PART + 4194304u)
#define WS_SP   (WS_ST + 32768u)
#define WS_YP   (WS_SP + 1179648u)
#define WS_END  (WS_YP + 1327104u)
#define WS_DBG  (8u << 20)

#define SPIN_MAX 20000
#define AUX_WQ 0
#define AUX_WUK 2
#define AUX_FFN 2
#define AUX_WUV 2
#define AUX_WO 0
#define KV_NT " nt"
#define DMA_SPLIT 12
#define DMA_WAIT 4
#define ZPRIME 1
#define FFN_DELAY 0
#define AUX_SC1 16
#define OOB 0x80000000u

#define STAMP_DECL u32 stamp_[16] = {}
#define STAMP(i) do { stamp_[i] = (u32)__builtin_amdgcn_s_memrealtime(); } while (0)
#define STAMP_FLUSH do { stamp_[15] = T ^ 0x5a5a1234u; if ((threadIdx.x & 63) == 0) { _Pragma("unroll") for (int i_ = 14; i_ >= 0; i_--) __hip_atomic_store(((u32*)(ws + WS_DBG)) + (blockIdx.x * 8 + (threadIdx.x >> 6)) * 16 + i_, stamp_[i_], __ATOMIC_RELAXED, __HIP_MEMORY_SCOPE_AGENT); \
   asm volatile("s_waitcnt vmcnt(0)" ::: "memory"); __hip_atomic_store(((u32*)(ws + WS_DBG)) + (blockIdx.x * 8 + (threadIdx.x >> 6)) * 16 + 15, stamp_[15], __ATOMIC_RELAXED, __HIP_MEMORY_SCOPE_AGENT); } \
   if (blockIdx.x == 56 && threadIdx.x < 64) dbg_print(ws, T); } while (0)
__device__ __forceinline__ void dbg_print(char* ws, unsigned T) {
    const int l = threadIdx.x;
    unsigned* d = (unsigned*)(ws + (8u << 20));
    for (int r = 0; r < 32; r++) { unsigned* p = d + (l * 32 + r) * 16 + 15;
        for (int it = 0; it < 200000; it++) { if (__hip_atomic_load(p, __ATOMIC_RELAXED, __HIP_MEMORY_SCOPE_AGENT) == (T ^ 0x5a5a1234u)) break; __builtin_amdgcn_s_sleep(8); } }
    unsigned ref0 = __hip_atomic_load(d, __ATOMIC_RELAXED, __HIP_MEMORY_SCOPE_AGENT);
    int t0 = 1 << 30;
    for (int r = 0; r < 32; r++) { int v = (int)(__hip_atomic_load(d + (l * 32 + r) * 16, __ATOMIC_RELAXED, __HIP_MEMORY_SCOPE_AGENT) - ref0); t0 = min(t0, v); }
    for (int o = 32; o; o >>= 1) t0 = min(t0, __shfl_xor(t0, o));
#pragma nounroll
    for (int s = 0; s < 10; s++) {
        int mn = 1 << 30, mx = -1, sum = 0, cnt = 0;
        for (int r = 0; r < 32; r++) { unsigned raw = __hip_atomic_load(d + (l * 32 + r) * 16 + s, __ATOMIC_RELAXED, __HIP_MEMORY_SCOPE_AGENT); if (!raw) continue; int v = (int)(raw - ref0) - t0; mn = min(mn, v); mx = max(mx, v); sum += v; cnt++; }
        for (int o = 16; o; o >>= 1) { mn = min(mn, __shfl_xor(mn, o)); mx = max(mx, __shfl_xor(mx, o)); sum += __shfl_xor(sum, o); cnt += __shfl_xor(cnt, o); }
        int av = cnt ? sum / cnt : 0;
        int mn2 = __shfl(mn, 32), mx2 = __shfl(mx, 32), av2 = __shfl(av, 32);
        if (l == 0) printf("s%d H %d %d %d A %d %d %d\n", s, mn, av, mx, mn2, av2, mx2);
    }
}

DI float lo2f(u32 v) { return __uint_as_float(v << 16); }
DI float hi2f(u32 v) { return __uint_as_float(v & 0xffff0000u); }
DI u32 pk2(float lo, float hi) { f32x2 v = {lo, hi}; return __builtin_bit_cast(u32, __builtin_convertvector(v, bf16x2)); }
DI float rbf(float f) { return lo2f(pk2(f, 0.f)); }
DI rsrc_t mkrs(const void* p, u32 bytes) { return __builtin_amdgcn_make_buffer_rsrc((void*)p, 0, (int)bytes, 0x00020000); }
template <int AUX> DI u32x4 ldb(rsrc_t rs, u32 voff, u32 soff) { return __builtin_amdgcn_raw_buffer_load_b128(rs, voff, soff, AUX); }
DI void stb(u32x4 v, rsrc_t rs, u32 voff, u32 soff) { __builtin_amdgcn_raw_buffer_store_b128(v, rs, voff, soff, AUX_SC1); }
DI void bar() { asm volatile("s_waitcnt lgkmcnt(0)\n\ts_barrier" ::: "memory"); }
DI void pin() { asm volatile("" ::: "memory"); }
DI f32x4 mf16(u32x4 a, u32x4 b, f32x4 c) { return __builtin_amdgcn_mfma_f32_16x16x32_bf16(__builtin_bit_cast(bf16x8, a), __builtin_bit_cast(bf16x8, b), c, 0, 0, 0); }
DI f32x16 mf32(u32x4 a, u32x4 b, f32x16 c) { return __builtin_amdgcn_mfma_f32_32x32x16_bf16(__builtin_bit_cast(bf16x8, a), __builtin_bit_cast(bf16x8, b), c, 0, 0, 0); }
DI float swap32max(float v) { auto r = __builtin_amdgcn_permlane32_swap(__float_as_uint(v), __float_as_uint(v), false, false); return fmaxf(__uint_as_float(r[0]), __uint_as_float(r[1])); }
DI float swap32sum(float v) { auto r = __builtin_amdgcn_permlane32_swap(__float_as_uint(v), __float_as_uint(v), false, false); return __uint_as_float(r[0]) + __uint_as_float(r[1]); }
DI float swap16max(float v) { auto r = __builtin_amdgcn_permlane16_swap(__float_as_uint(v), __float_as_uint(v), false, false); return fmaxf(__uint_as_float(r[0]), __uint_as_float(r[1])); }
DI float swap16sum(float v) { auto r = __builtin_amdgcn_permlane16_swap(__float_as_uint(v), __float_as_uint(v), false, false); return __uint_as_float(r[0]) + __uint_as_float(r[1]); }
template <int CTRL> DI float dpp(float v) { return __uint_as_float(__builtin_amdgcn_update_dpp(0, __float_as_uint(v), CTRL, 0xf, 0xf, true)); }
DI float wave_sum(float v) {
    v += dpp<0xb1>(v);
    v += dpp<0x4e>(v);
    v += dpp<0x141>(v);
    v += dpp<0x140>(v);
    v = swap16sum(v);
    return swap32sum(v);
}
DI void tr8(const u32x4 (&r)[8], u32x4 (&t)[8]) {
#pragma unroll
    for (int jj = 0; jj < 4; jj++) {
#pragma unroll
        for (int p = 0; p < 4; p++) {
            t[2 * jj][p] = __builtin_amdgcn_perm(r[2 * p + 1][jj], r[2 * p][jj], 0x05040100u);
            t[2 * jj + 1][p] = __builtin_amdgcn_perm(r[2 * p + 1][jj], r[2 * p][jj], 0x07060302u);
        }
    }
}
template <int AUX> DI void ld8(u32x4 (&r)[8], rsrc_t rs, u32 voff, u32 soff, u32 stride) {
#pragma unroll
    for (int i = 0; i < 8; i++) r[i] = ldb<AUX>(rs, voff, soff + i * stride);
}
DI void gemv8(const u32x4 (&r)[8], u32x4 a, f32x4 (&acc)[8]) {
    u32x4 t[8]; tr8(r, t);
#pragma unroll
    for (int j = 0; j < 8; j++) acc[j] = mf16(a, t[j], acc[j]);
}
DI u32 tag12(float v, u32 tg) { return ((__float_as_uint(v) + 0x800u) & 0xfffff000u) | tg; }
DI u32x4 lds128(const char* p) { return *(const u32x4*)p; }

#define XN_STRIDE 1216
#define H_XN   0
#define H_RED  5120
#define H_QB   (H_RED + 20480)
#define H_HB   (H_QB + 1280)
#define H_FIN  (H_HB + 512)

DI void h_role(char* smem, const u16* x, const u16* wq, const u16* wuk, const u16* wg, const u16* wu, const u16* wd,
               u16* out, char* ws) {
    const int tid = threadIdx.x, l = tid & 63, w = __builtin_amdgcn_readfirstlane(tid >> 6);
    const int k = blockIdx.x, xc = k & 7, ii = k >> 3, h = 4 * xc + (ii >> 2), cq = ii & 3;
    const int n = l & 15, g = l >> 4;
    STAMP_DECL;
    STAMP(0);
    const rsrc_t rws = mkrs(ws, WS_END);
    const u32 E = __hip_atomic_load((const u32*)ws, __ATOMIC_RELAXED, __HIP_MEMORY_SCOPE_AGENT);
    u32x2 xv[3];
    {
        const rsrc_t rx = mkrs(x, 4 * 576 * 2);
        const u32 vo = (w < 4) ? (u32)(w * 1152 + l * 8) : OOB;
        xv[0] = __builtin_amdgcn_raw_buffer_load_b64(rx, vo, 0, 0);
        xv[1] = __builtin_amdgcn_raw_buffer_load_b64(rx, vo, 512, 0);
        xv[2] = __builtin_amdgcn_raw_buffer_load_b64(rx, l < 16 ? vo : OOB, 1024, 0);
    }
    const rsrc_t rq = mkrs(wq, 576 * 4096 * 2);
    const int rot = (9 * cq) >> 1;
    int b0 = w + rot; if (b0 >= 18) b0 -= 18;
    int b1 = w + 8 + rot; if (b1 >= 18) b1 -= 18;
    int b2 = w + 16 + rot; if (b2 >= 18) b2 -= 18;
    const u32 vq = (u32)(8 * g * 8192 + h * 256 + n * 16);
    u32x4 r0[8], r1[8], r2[8];
    ld8<AUX_WQ>(r0, rq, vq, b0 * 262144, 8192);
    ld8<AUX_WQ>(r1, rq, vq, b1 * 262144, 8192);
    ld8<AUX_WQ>(r2, rq, w < 2 ? vq : OOB, b2 * 262144, 8192);
    u32x4 uk[4];
    {
        const rsrc_t ru = mkrs(wuk, 512 * 4096 * 2);
        const u32 vo = (u32)(((128 * cq + 16 * w + n) * 32 + h) * 256 + g * 16);
#pragma unroll
        for (int s = 0; s < 4; s++) uk[s] = ldb<AUX_WUK>(ru, vo + s * 64, 0);
    }
    {
        const u32x4 ones = {~0u, ~0u, ~0u, ~0u};
#pragma unroll
        for (int i = 0; i < 4; i++) stb(ones, rws, (u32)(tid * 16 + i * 8192), WS_PART + (h * 4 + cq) * 32768);
        if (tid < 16) stb(ones, rws, (u32)(tid * 16), WS_ST + h * 1024 + cq * 256);
    }
    pin();
    STAMP(8);
    if (w < 4) {
        float v[12];
#pragma unroll
        for (int i = 0; i < 3; i++) { v[4 * i] = lo2f(xv[i][0]); v[4 * i + 1] = hi2f(xv[i][0]); v[4 * i + 2] = lo2f(xv[i][1]); v[4 * i + 3] = hi2f(xv[i][1]); }
        float s = 0;
#pragma unroll
        for (int i = 0; i < 12; i++) s += v[i];
        const float mf = wave_sum(s) * (1.0f / 576);
        const float m = rbf(mf);
        float q = 0;
#pragma unroll
        for (int i = 0; i < 8; i++) { float d = v[i] - mf; q += d * d; }
        if (l < 16) {
#pragma unroll
            for (int i = 8; i < 12; i++) { float d = v[i] - mf; q += d * d; }
        }
        const float var = rbf(wave_sum(q) * (1.0f / 576));
        const float den = rbf(sqrtf(rbf(var + 1e-5f)));
#pragma unroll
        for (int i = 0; i < 3; i++) {
            u32x2 o;
            o[0] = pk2(rbf(v[4 * i] - m) / den, rbf(v[4 * i + 1] - m) / den);
            o[1] = pk2(rbf(v[4 * i + 2] - m) / den, rbf(v[4 * i + 3] - m) / den);
            if (i < 2 || l < 16) *(u32x2*)(smem + H_XN + w * XN_STRIDE + i * 512 + l * 8) = o;
        }
    }
    bar();
    STAMP(1);
    {
        f32x4 acc[8];
#pragma unroll
        for (int j = 0; j < 8; j++) acc[j] = f32x4{0, 0, 0, 0};
        const char* xa = smem + H_XN + (n >> 2) * XN_STRIDE + g * 16;
        gemv8(r0, lds128(xa + b0 * 64), acc);
        gemv8(r1, lds128(xa + b1 * 64), acc);
        if (w < 2) gemv8(r2, lds128(xa + b2 * 64), acc);
        float* red = (float*)(smem + H_RED) + (w * 4 + g) * 128 + 4 * n;
        *(f32x4*)red = f32x4{acc[0][0], acc[1][0], acc[2][0], acc[3][0]};
        *(f32x4*)(red + 64) = f32x4{acc[4][0], acc[5][0], acc[6][0], acc[7][0]};
    }
    STAMP(2);
    bar();
    {
        const float* red = (const float*)(smem + H_RED) + tid;
        float s = 0;
#pragma unroll
        for (int i = 0; i < 8; i++) s += red[i * 512];
        const int b = tid >> 7, idx = tid & 127, d = 8 * ((idx >> 2) & 15) + 4 * (idx >> 6) + (idx & 3);
        *(u16*)(smem + H_QB + b * 320 + d * 2) = (u16)pk2(s, 0.f);
    }
    bar();
    const u32 T = E + 1;
    {
        f32x4 acc = {0, 0, 0, 0};
        const char* qa = smem + H_QB + (n & 3) * 320 + g * 16;
#pragma unroll
        for (int s = 0; s < 4; s++) acc = mf16(uk[s], lds128(qa + s * 64), acc);
        const float sc = 0.08838834764831845f * 1.4426950408889634f;
        const u32 tg = T & 0xffffu;
        u32x4 o;
#pragma unroll
        for (int r = 0; r < 4; r++) o[r] = (pk2(0.f, acc[r] * sc) & 0xffff0000u) | tg;
        asm volatile("s_waitcnt vmcnt(0)" ::: "memory");
        if (n < 4) stb(o, rws, (u32)(((n * 4 + cq) * 32 + h) * 512 + (16 * w + 4 * g) * 4), WS_QA);
    }
    STAMP(3);
    const u32 tg12 = T & 0xfffu;
    const int nunits = (ii < 2) ? 2 : 1;
    for (int un = 0; un < nunits; un++) {
        const int u = un == 0 ? 16 * xc + ii : 128 + 2 * xc + ii;
        const rsrc_t rg = mkrs(w < 4 ? wg : wu, 576 * 4608 * 2);
        u32x4 f0[8], f1[8];
        const u32 vg = (u32)(((n >> 2) * 32 + 8 * g) * 9216 + (n & 3) * 16 + u * 64);
        ld8<AUX_FFN>(f0, rg, vg, (w & 3) * 128 * 9216, 9216);
        {
            const rsrc_t rd = mkrs(w < 5 ? wd : (w == 5 ? wg : wu), 576 * 4608 * 2);
            const u32 vd = (w == 4 && n >= 8) ? OOB : (u32)(8 * g * 1152 + n * 16);
            const u32 vh = (n >= 8 || w == 7) ? OOB : vg;
            ld8<AUX_FFN>(f1, rd, w < 5 ? vd : vh, w < 5 ? u * 32 * 1152 + w * 256 : 512 * 9216, w < 5 ? 1152 : 9216);
        }
        pin();
        const char* xa = smem + H_XN + (n & 3) * XN_STRIDE + ((n >> 2) * 32 + 8 * g) * 2;
        float* red = (float*)(smem + H_RED);
        const bool dv = (g == (n >> 2));
        {
            f32x4 acc[8];
#pragma unroll
            for (int j = 0; j < 8; j++) acc[j] = f32x4{0, 0, 0, 0};
            gemv8(f0, lds128(xa + (w & 3) * 256), acc);
            if (dv) {
                float* p = red + (((w >> 2) * 5 + (w & 3)) * 4 + g) * 128 + (n & 3) * 8;
#pragma unroll
                for (int b = 0; b < 4; b++) {
                    *(f32x4*)(p + b * 32) = f32x4{acc[0][b], acc[1][b], acc[2][b], acc[3][b]};
                    *(f32x4*)(p + b * 32 + 4) = f32x4{acc[4][b], acc[5][b], acc[6][b], acc[7][b]};
                }
            }
        }
        if (w == 5 || w == 6) {
            f32x4 acc[8];
#pragma unroll
            for (int j = 0; j < 8; j++) acc[j] = f32x4{0, 0, 0, 0};
            u32x4 a = lds128(smem + H_XN + (n & 3) * XN_STRIDE + (512 + (((n >> 2) & 1) * 32 + 8 * g)) * 2);
            gemv8(f1, a, acc);
            if (dv) {
                float* p = red + (((w - 5) * 5 + 4) * 4 + g) * 128 + (n & 3) * 8;
#pragma unroll
                for (int b = 0; b < 4; b++) {
                    *(f32x4*)(p + b * 32) = f32x4{acc[0][b], acc[1][b], acc[2][b], acc[3][b]};
                    *(f32x4*)(p + b * 32 + 4) = f32x4{acc[4][b], acc[5][b], acc[6][b], acc[7][b]};
                }
            }
        }
        bar();
        if (tid < 128) {
            float gs = 0, us = 0;
#pragma unroll
            for (int i = 0; i < 18; i++) { gs += red[i * 128 + tid]; us += red[(20 + i) * 128 + tid]; }
            const float gg = rbf(gs), uu = rbf(us);
            const float inner = 0.7978845608028654f * (gg + 0.044715f * gg * gg * gg);
            const float ge = rbf(0.5f * gg * (1.0f + tanhf(inner)));
            *(u16*)(smem + H_HB + (tid >> 5) * 80 + (tid & 31) * 2) = (u16)pk2(ge * uu, 0.f);
        }
        bar();
        if (w < 5) {
            f32x4 acc[8];
#pragma unroll
            for (int j = 0; j < 8; j++) acc[j] = f32x4{0, 0, 0, 0};
            gemv8(f1, lds128(smem + H_HB + (n >> 2) * 80 + g * 16), acc);
            if (w < 4 || n < 8) {
                const u32 vo = (u32)(((g * 18 + 4 * w + (n >> 2)) * 144 + u) * 128 + (n & 3) * 32);
                stb(u32x4{tag12(acc[0][0], tg12), tag12(acc[1][0], tg12), tag12(acc[2][0], tg12), tag12(acc[3][0], tg12)}, rws, vo, WS_YP);
                stb(u32x4{tag12(acc[4][0], tg12), tag12(acc[5][0], tg12), tag12(acc[6][0], tg12), tag12(acc[7][0], tg12)}, rws, vo + 16, WS_YP);
            }
        }
    }
    STAMP(4);
    if (k >= 56) {
        const int r = k - 56, b = r / 18, m0 = (r - b * 18) * 32;
        float xres = 0.f;
        if (tid < 32) xres = lo2f(x[b * 576 + m0 + tid]);
        f32x4 ys = {0, 0, 0, 0}, ss = {0, 0, 0, 0};
        {
            const u32 vo = (u32)(r * 144 * 128 + tid * 16);
            for (int it = 0; it < SPIN_MAX; it++) {
                u32x4 a = ldb<AUX_SC1>(rws, vo, WS_YP), c = ldb<AUX_SC1>(rws, vo + 8192, WS_YP);
                u32x4 d = ldb<AUX_SC1>(rws, tid < 128 ? vo + 16384 : OOB, WS_YP);
                u32 bad = 0;
#pragma unroll
                for (int e = 0; e < 4; e++) bad |= (a[e] ^ tg12) | (c[e] ^ tg12) | (tid < 128 ? (d[e] ^ tg12) : 0u);
                if (__all((bad & 0xfffu) == 0) || it == SPIN_MAX - 1) {
#pragma unroll
                    for (int e = 0; e < 4; e++) ys[e] = __uint_as_float(a[e] & 0xfffff000u) + __uint_as_float(c[e] & 0xfffff000u) + __uint_as_float(d[e] & 0xfffff000u);
                    break;
                }
                __builtin_amdgcn_s_sleep(4);
            }
        }
        STAMP(5);
        {
            const u32 vo = (u32)(r * 128 * 128 + tid * 16);
            for (int it = 0; it < SPIN_MAX; it++) {
                u32x4 a = ldb<AUX_SC1>(rws, vo, WS_SP), c = ldb<AUX_SC1>(rws, vo + 8192, WS_SP);
                u32 bad = 0;
#pragma unroll
                for (int e = 0; e < 4; e++) bad |= (a[e] ^ tg12) | (c[e] ^ tg12);
                if (__all((bad & 0xfffu) == 0) || it == SPIN_MAX - 1) {
#pragma unroll
                    for (int e = 0; e < 4; e++) ss[e] = __uint_as_float(a[e] & 0xfffff000u) + __uint_as_float(c[e] & 0xfffff000u);
                    break;
                }
                __builtin_amdgcn_s_sleep(1);
            }
        }
        STAMP(6);
#pragma unroll
        for (int e = 0; e < 4; e++) {
            ys[e] += dpp<0x128>(ys[e]); ss[e] += dpp<0x128>(ss[e]);
            ys[e] = swap32sum(swap16sum(ys[e])); ss[e] = swap32sum(swap16sum(ss[e]));
        }
        float* fin = (float*)(smem + H_FIN);
        if (l < 8) { *(f32x4*)(fin + w * 32 + l * 4) = ys; *(f32x4*)(fin + 256 + w * 32 + l * 4) = ss; }
        bar();
        if (tid < 32) {
            float y = 0, s = 0;
#pragma unroll
            for (int i = 0; i < 8; i++) { y += fin[i * 32 + tid]; s += fin[256 + i * 32 + tid]; }
            y = rbf(y); s = rbf(s);
            const float gt = rbf(y / (1.0f + __expf(-y)));
            out[b * 576 + m0 + tid] = (u16)pk2(xres + rbf(s * gt), 0.f);
        }
        if (k == 56 && tid == 0) __hip_atomic_store((u32*)ws, T, __ATOMIC_RELAXED, __HIP_MEMORY_SCOPE_AGENT);
        STAMP(7);
    }
    STAMP_FLUSH;
}

#define A_Q 131072
DI void dma_row(const u16* gbase, u32 voff, u32 ldsaddr) {
    u32 keep;
    asm volatile("s_mov_b32 %0, m0\n\ts_mov_b32 m0, %3\n\ts_nop 0\n\tglobal_load_lds_dwordx4 %1, %2" KV_NT "\n\ts_mov_b32 m0, %0"
                 : "=&s"(keep) : "v"(voff), "s"(gbase), "s"(ldsaddr) : "memory");
}
template <int OFF> DI void ld_sc1_asm(u32x4& r, u32 voff, const char* base) {
    asm volatile("global_load_dwordx4 %0, %1, %2 offset:%3 sc1" : "=&v"(r) : "v"(voff), "s"(base), "n"(OFF) : "memory");
}

DI void a_weights(u32x4 (&w0)[8], u32x4 (&w1)[8], u32x4 (&w2)[8], const u16* wuv, const u16* wo, int w, int h, int al, int n, int g) {
    const int kb = w & 3;
    const rsrc_t ro = mkrs(wo, 4096 * 576 * 2);
    const u32 vo = (u32)(8 * g * 1152 + n * 16);
    const u32 so = (u32)((h * 128 + 32 * kb) * 1152);
    if (w < 4) {
        const rsrc_t rv = mkrs(wuv, 512 * 4096 * 2);
        ld8<AUX_WUV>(w0, rv, (u32)(8 * g * 8192 + h * 256 + n * 16), (128 * al + 32 * kb) * 8192, 8192);
    } else {
        ld8<AUX_WO>(w0, ro, vo, so, 1152);
    }
    ld8<AUX_WO>(w1, ro, vo, so + (1 + (w >> 2)) * 256, 1152);
    ld8<AUX_WO>(w2, ro, (w >= 4 && n >= 8) ? OOB : vo, so + (3 + (w >> 2)) * 256, 1152);
}

DI void a_role(char* smem, const u16* ckv, const u16* wuv, const u16* wo, char* ws) {
    const int tid = threadIdx.x, l = tid & 63, w = __builtin_amdgcn_readfirstlane(tid >> 6);
    const int a = blockIdx.x - 128, xc = a & 7, ii = a >> 3;
    const int b = xc >> 1, chunk = (xc & 1) * 16 + ii;
    const int h = 4 * xc + (ii >> 2), al = ii & 3;
    const int n = l & 15, g = l >> 4, hh = l >> 5;
    STAMP_DECL;
    STAMP(0);
    const rsrc_t rws = mkrs(ws, WS_END);
    u32 E;
    asm volatile("global_load_dword %0, %1, %2 sc1" : "=&v"(E) : "v"(0u), "s"(ws) : "memory");
    const u32 lds0 = (u32)(size_t)(LAS char*)smem;
    const u16* kb0 = ckv + ((size_t)b * 4096 + chunk * 128 + 16 * w) * 512;
    const u32 ldsw = lds0 + w * 16384;
#pragma unroll
    for (int i = 0; i < DMA_SPLIT; i++) dma_row(kb0, (u32)(((l ^ (((i & 3) << 2) | (i >> 2))) << 4) + i * 1024), ldsw + i * 1024);
    asm volatile("s_waitcnt vmcnt(%1)" : "+v"(E) : "n"(DMA_WAIT) : "memory");
    const u32 T = E + 1;
    {
        const char* qsrc = ws + WS_QA + b * 65536 + w * 8192;
        const u32 vq = l * 16 + 4096;
        const u32 tg = T & 0xffffu;
        u32x4 q[8];
        STAMP(1);
        bool first = true;
        for (int it = 0; it < SPIN_MAX; it++) {
            ld_sc1_asm<-4096>(q[0], vq, qsrc); ld_sc1_asm<-3072>(q[1], vq, qsrc);
            ld_sc1_asm<-2048>(q[2], vq, qsrc); ld_sc1_asm<-1024>(q[3], vq, qsrc);
            ld_sc1_asm<0>(q[4], vq, qsrc); ld_sc1_asm<1024>(q[5], vq, qsrc);
            ld_sc1_asm<2048>(q[6], vq, qsrc); ld_sc1_asm<3072>(q[7], vq, qsrc);
            if (first) {
                first = false;
#pragma unroll
                for (int i = DMA_SPLIT; i < 16; i++) dma_row(kb0, (u32)(((l ^ (((i & 3) << 2) | (i >> 2))) << 4) + i * 1024), ldsw + i * 1024);
                asm volatile("s_waitcnt vmcnt(%8)" : "+v"(q[0]), "+v"(q[1]), "+v"(q[2]), "+v"(q[3]), "+v"(q[4]), "+v"(q[5]), "+v"(q[6]), "+v"(q[7]) : "n"(16 - DMA_SPLIT) : "memory");
            } else {
                asm volatile("s_waitcnt vmcnt(0)" : "+v"(q[0]), "+v"(q[1]), "+v"(q[2]), "+v"(q[3]), "+v"(q[4]), "+v"(q[5]), "+v"(q[6]), "+v"(q[7]) :: "memory");
            }
            u32 bad = 0;
#pragma unroll
            for (int i = 0; i < 8; i++) bad |= (q[i][0] ^ tg) | (q[i][1] ^ tg) | (q[i][2] ^ tg) | (q[i][3] ^ tg);
            if (__all((bad & 0xffffu) == 0)) break;
            __builtin_amdgcn_s_sleep(1);
        }
        STAMP(2);
        const int z = (l >> 1) & 7;
        char* qd = smem + A_Q + (8 * (w >> 1) + ((l & 31) >> 2)) * 1024 + ((l >> 1) & 1) * 512 + (l & 1) * 8;
#pragma unroll
        for (int i = 0; i < 8; i++) {
            const int hq = 16 * (w & 1) + 2 * i + hh;
            u32x2 o = {__builtin_amdgcn_perm(q[i][1], q[i][0], 0x07060302u), __builtin_amdgcn_perm(q[i][3], q[i][2], 0x07060302u)};
            *(u32x2*)(qd + ((hq ^ z) << 4)) = o;
        }
    }
    u32x4 w0[8], w1[8], w2[8];
    asm volatile("s_waitcnt vmcnt(0)" ::: "memory");
    a_weights(w0, w1, w2, wuv, wo, w, h, al, n, g);
    pin();
    bar();
    STAMP(3);
    f32x16 sacc;
    float mx = 0.f;
    if (w < 4) {
#pragma unroll
        for (int r = 0; r < 16; r++) sacc[r] = 0.f;
        const int row = 32 * w + (l & 31);
        const int f = ((row & 3) << 2) | ((row >> 2) & 3);
        const char* kp = smem + row * 1024;
        const int fx = (f ^ hh) << 4;
        const char* qp = smem + A_Q + hh * 512;
#pragma unroll
        for (int s = 0; s < 32; s++) {
            const u32x4 ka = lds128(kp + ((32 * (s & 7)) ^ fx) + 256 * (s >> 3));
            const u32x4 qb = lds128(qp + s * 1024 + (((l & 31) ^ ((s & 3) * 2 + hh)) << 4));
            sacc = mf32(ka, qb, sacc);
        }
        mx = sacc[0];
#pragma unroll
        for (int r = 1; r < 16; r++) mx = fmaxf(mx, sacc[r]);
        mx = swap32max(mx);
    }
    bar();
    float* aux = (float*)(smem + A_Q);
    if (w < 4 && l < 32) aux[w * 32 + l] = mx;
    bar();
    if (w < 4) {
        const int hq = l & 31;
        const float M = fmaxf(fmaxf(aux[hq], aux[32 + hq]), fmaxf(aux[64 + hq], aux[96 + hq]));
        float p[16], ls = 0.f;
#pragma unroll
        for (int r = 0; r < 16; r++) { p[r] = __builtin_amdgcn_exp2f(sacc[r] - M); ls += p[r]; }
        ls = swap32sum(ls);
        if (l < 32) aux[128 + w * 32 + l] = ls;
        char* pd = smem + A_Q + 1024 + (2 * w) * 1024 + l * 16;
        *(u32x4*)pd = u32x4{pk2(p[0], p[1]), pk2(p[2], p[3]), pk2(p[4], p[5]), pk2(p[6], p[7])};
        *(u32x4*)(pd + 1024) = u32x4{pk2(p[8], p[9]), pk2(p[10], p[11]), pk2(p[12], p[13]), pk2(p[14], p[15])};
    }
    bar();
    STAMP(4);
    if (w == 4 && l < 32) {
        const float M = fmaxf(fmaxf(aux[l], aux[32 + l]), fmaxf(aux[64 + l], aux[96 + l]));
        const float L = (aux[128 + l] + aux[160 + l]) + (aux[192 + l] + aux[224 + l]);
        u32x2 o = {min(__float_as_uint(M), 0xfffffffeu), min(__float_as_uint(L), 0xfffffffeu)};
        __builtin_amdgcn_raw_buffer_store_b64(o, rws, (u32)(l * 1024 + (b * 32 + chunk) * 8), WS_ST, AUX_SC1);
    }
    {
        f32x16 o0, o1;
#pragma unroll
        for (int r = 0; r < 16; r++) { o0[r] = 0.f; o1[r] = 0.f; }
        const int q4 = (l & 15) >> 2, p4 = l & 3, blk = (l >> 4) & 1;
        const char* vb = smem + (4 * hh + q4) * 1024 + (w >> 1) * 256 + (p4 >> 1) * 8;
        const int cl = 8 * (w & 1) + 2 * blk + (p4 & 1);
        const char* pp = smem + A_Q + 1024 + l * 16;
#pragma unroll
        for (int ks = 0; ks < 8; ks++) {
            const u32x4 pb = lds128(pp + ks * 1024);
            u32x4 va[2];
#pragma unroll
            for (int cb = 0; cb < 2; cb++) {
#pragma unroll
                for (int t = 0; t < 2; t++) {
                    const int f = (q4 << 2) | (hh + 2 * t);
                    const char* ad = vb + ks * 16384 + t * 8192 + (((cl + 4 * cb) ^ f) << 4);
                    s16x4 v = __builtin_amdgcn_ds_read_tr16_b64_v4i16((LAS s16x4*)ad);
                    u32x2 vv = __builtin_bit_cast(u32x2, v);
                    va[cb][2 * t] = vv[0]; va[cb][2 * t + 1] = vv[1];
                }
            }
            o0 = mf32(va[0], pb, o0);
            o1 = mf32(va[1], pb, o1);
        }
        STAMP(5);
        const u32 vo = (u32)((((l & 31) * 4 + (w >> 1)) * 128 + b * 32 + chunk) * 256 + (64 * (w & 1) + 8 * hh) * 2);
#pragma unroll
        for (int i = 0; i < 2; i++) {
            u32x4 s0, s1;
#pragma unroll
            for (int e = 0; e < 4; e++) {
                s0[e] = min(pk2(o0[8 * i + 2 * e], o0[8 * i + 2 * e + 1]), 0xfffffffeu);
                s1[e] = min(pk2(o1[8 * i + 2 * e], o1[8 * i + 2 * e + 1]), 0xfffffffeu);
            }
            stb(s0, rws, vo + i * 32, WS_PART);
            stb(s1, rws, vo + 64 + i * 32, WS_PART);
        }
    }
    STAMP(6);
    u32x4 t0[8], t1[8], t2[8];
    tr8(w0, t0); tr8(w1, t1); tr8(w2, t2);
#pragma unroll
    for (int j = 0; j < 8; j++) asm volatile("" : "+v"(t0[j]), "+v"(t1[j]), "+v"(t2[j]));
    STAMP(7);
#define D_WT (A_Q + 16384)
#define D_OB 4096
#define D_CX 8192
#define D_SR 16384
    u32x4 pt[8];
    if (w < 4) {
        const u32 vo = (u32)((w * 32 + 8 * g) * 256 + n * 16);
        const u32 so = WS_PART + (h * 4 + al) * 32768;
        u32x4 pr[8];
        for (int it = 0; it < SPIN_MAX; it++) {
#pragma unroll
            for (int i = 0; i < 8; i++) pr[i] = ldb<AUX_SC1>(rws, vo + i * 256, so);
            u32 mxv = 0;
#pragma unroll
            for (int i = 0; i < 8; i++) mxv = max(max(mxv, max(pr[i][0], pr[i][1])), max(pr[i][2], pr[i][3]));
            if (__all(mxv != 0xffffffffu)) break;
            __builtin_amdgcn_s_sleep(1);
        }
        tr8(pr, pt);
    }
    u32x4 wt = {0, 0, 0, 0};
    if (w >= 4) {
        const u32 vo = (u32)(h * 1024 + ((w - 4) * 32 + 8 * g) * 8);
        u32x4 sr[4];
        for (int it = 0; it < SPIN_MAX; it++) {
#pragma unroll
            for (int i = 0; i < 4; i++) sr[i] = ldb<AUX_SC1>(rws, vo + i * 16, WS_ST);
            u32 mxv = 0;
#pragma unroll
            for (int i = 0; i < 4; i++) mxv = max(max(mxv, max(sr[i][0], sr[i][1])), max(sr[i][2], sr[i][3]));
            if (__all(mxv != 0xffffffffu)) break;
            __builtin_amdgcn_s_sleep(1);
        }
        float m[8], ll[8];
#pragma unroll
        for (int i = 0; i < 4; i++) { m[2 * i] = __uint_as_float(sr[i][0]); ll[2 * i] = __uint_as_float(sr[i][1]); m[2 * i + 1] = __uint_as_float(sr[i][2]); ll[2 * i + 1] = __uint_as_float(sr[i][3]); }
        float M = m[0];
#pragma unroll
        for (int i = 1; i < 8; i++) M = fmaxf(M, m[i]);
        M = swap32max(swap16max(M));
        float L = 0.f;
#pragma unroll
        for (int i = 0; i < 8; i++) { m[i] = __builtin_amdgcn_exp2f(m[i] - M); L += m[i] * ll[i]; }
        L = swap32sum(swap16sum(L));
        const float inv = 1.0f / L;
#pragma unroll
        for (int p = 0; p < 4; p++) {
            const float a0 = m[2 * p] * inv, a1 = m[2 * p + 1] * inv;
            const u32 hi = pk2(a0, a1);
            const u32 lo = pk2(a0 - lo2f(hi), a1 - hi2f(hi));
            wt[p] = n == 0 ? hi : (n == 1 ? lo : 0u);
        }
    }
    if (w >= 4) *(u32x4*)(smem + D_WT + (w - 4) * 1024 + l * 16) = wt;
    bar();
    STAMP(8);
    if (w < 4) {
        const u32x4 aw = lds128(smem + D_WT + w * 1024 + l * 16);
        float o[8];
#pragma unroll
        for (int j = 0; j < 8; j++) { f32x4 d = mf16(aw, pt[j], f32x4{0, 0, 0, 0}); o[j] = d[0] + d[1]; }
        if (g == 0) *(u32x4*)(smem + D_OB + w * 320 + n * 16) = u32x4{pk2(o[0], o[1]), pk2(o[2], o[3]), pk2(o[4], o[5]), pk2(o[6], o[7])};
    }
    bar();
    if (w < 4) {
        const u32x4 ao = lds128(smem + D_OB + (n >> 2) * 320 + (32 * w + 8 * g) * 2);
        f32x4 d[8];
#pragma unroll
        for (int j = 0; j < 8; j++) d[j] = mf16(ao, t0[j], f32x4{0, 0, 0, 0});
        float* cx = (float*)(smem + D_CX) + (w * 4 + g) * 128 + 8 * n;
        *(f32x4*)cx = f32x4{d[0][0], d[1][0], d[2][0], d[3][0]};
        *(f32x4*)(cx + 4) = f32x4{d[4][0], d[5][0], d[6][0], d[7][0]};
    }
    bar();
    {
        const int kb = w & 3;
        const float* cx = (const float*)(smem + D_CX) + (n >> 2) * 128 + 32 * kb + 8 * g;
        f32x4 lo = *(const f32x4*)cx, hi = *(const f32x4*)(cx + 4);
#pragma unroll
        for (int i = 1; i < 4; i++) { lo += *(const f32x4*)(cx + i * 512); hi += *(const f32x4*)(cx + i * 512 + 4); }
        const u32x4 ac = {pk2(lo[0], lo[1]), pk2(lo[2], lo[3]), pk2(hi[0], hi[1]), pk2(hi[2], hi[3])};
        float* sr = (float*)(smem + D_SR) + (kb * 4 + g) * 640 + 8 * n;
        f32x4 d[8];
        if (w >= 4) {
#pragma unroll
            for (int j = 0; j < 8; j++) d[j] = mf16(ac, t0[j], f32x4{0, 0, 0, 0});
            *(f32x4*)sr = f32x4{d[0][0], d[1][0], d[2][0], d[3][0]};
            *(f32x4*)(sr + 4) = f32x4{d[4][0], d[5][0], d[6][0], d[7][0]};
        }
#pragma unroll
        for (int j = 0; j < 8; j++) d[j] = mf16(ac, t1[j], f32x4{0, 0, 0, 0});
        float* s1 = sr + (1 + (w >> 2)) * 128;
        *(f32x4*)s1 = f32x4{d[0][0], d[1][0], d[2][0], d[3][0]};
        *(f32x4*)(s1 + 4) = f32x4{d[4][0], d[5][0], d[6][0], d[7][0]};
#pragma unroll
        for (int j = 0; j < 8; j++) d[j] = mf16(ac, t2[j], f32x4{0, 0, 0, 0});
        float* s2 = sr + (3 + (w >> 2)) * 128;
        *(f32x4*)s2 = f32x4{d[0][0], d[1][0], d[2][0], d[3][0]};
        *(f32x4*)(s2 + 4) = f32x4{d[4][0], d[5][0], d[6][0], d[7][0]};
    }
    bar();
    {
        const u32 tg12 = T & 0xfffu;
#pragma unroll
        for (int i = 0; i < 2; i++) {
            const int ch = tid + 512 * i;
            if (ch < 576) {
                const int bb = ch / 144, m = (ch - bb * 144) * 4;
                const float* sr = (const float*)(smem + D_SR) + bb * 640 + m;
                f32x4 s = *(const f32x4*)sr;
#pragma unroll
                for (int q = 1; q < 4; q++) s += *(const f32x4*)(sr + q * 2560);
                stb(u32x4{tag12(s[0], tg12), tag12(s[1], tg12), tag12(s[2], tg12), tag12(s[3], tg12)}, rws,
                    (u32)(((bb * 18 + (m >> 5)) * 128 + a) * 128 + (m & 31) * 4), WS_SP);
            }
        }
    }
    STAMP(9);
    STAMP_FLUSH;
}

__global__ __launch_bounds__(512, 2) void fused_decode(const u16* x, const u16* ckv, const u16* wq, const u16* wuk, const u16* wuv,
                                                       const u16* wo, const u16* wg, const u16* wu, const u16* wd, u16* out, char* ws) {
    extern __shared__ __attribute__((aligned(16))) char smem[];
    if (blockIdx.x < 128) h_role(smem, x, wq, wuk, wg, wu, wd, out, ws);
    else a_role(smem, ckv, wuv, wo, ws);
}

extern "C" void kernel_launch(void* const* d_in, const int* in_sizes, int n_in, void* d_out, int out_size, void* d_ws, size_t ws_size,
                              hipStream_t stream) {
    (void)in_sizes; (void)n_in; (void)out_size; (void)ws_size;
    hipFuncSetAttribute((const void*)fused_decode, hipFuncAttributeMaxDynamicSharedMemorySize, 163840);
    fused_decode<<<256, 512, 163840, stream>>>((const u16*)d_in[0], (const u16*)d_in[1], (const u16*)d_in[2], (const u16*)d_in[3],
                                               (const u16*)d_in[4], (const u16*)d_in[5], (const u16*)d_in[7], (const u16*)d_in[8],
                                               (const u16*)d_in[9], (u16*)d_out, (char*)d_ws);
}
```

```cpp
#include <hip/hip_runtime.h>
typedef unsigned short u16;
typedef unsigned int u32;
typedef unsigned long long u64;
typedef u32 u32x4 __attribute__((ext_vector_type(4)));
typedef u32 u32x2 __attribute__((ext_vector_type(2)));
typedef float f32x4 __attribute__((ext_vector_type(4)));
typedef float f32x2 __attribute__((ext_vector_type(2)));
typedef float f32x16 __attribute__((ext_vector_type(16)));
typedef __bf16 bf16x8 __attribute__((ext_vector_type(8)));
typedef __bf16 bf16x2 __attribute__((ext_vector_type(2)));
typedef short s16x4 __attribute__((ext_vector_type(4)));
typedef __amdgpu_buffer_rsrc_t rsrc_t;
#define DI __device__ __forceinline__
#define LAS __attribute__((address_space(3)))

#define WS_QA   4096u
#define WS_PART (WS_QA + 262144u)
#define WS_ST   (WS_PART + 4194304u)
#define WS_SP   (WS_ST + 32768u)
#define WS_YP   (WS_SP + 1179648u)
#define WS_CL   (WS_YP + 1327104u)
#define WS_END  (WS_CL + 16384u)
#define WS_DBG  (8u << 20)

#define SPIN_MAX 20000
#define AUX_WQ 0
#define AUX_WUK 0
#define AUX_FFN 2
#define AUX_WUV 2
#define AUX_WO 0
#define KV_NT " nt"
#define DMA_SPLIT 12
#define DMA_WAIT 4
#define ZPRIME 1
#define FFN_DELAY 0
#define A_DELAY 0
#define CLR_EARLY 8
#define POLL_SLEEP 1
#define HORD 1
#define AUX_SC1 16
#define OOB 0x80000000u

#define STAMP_DECL
#define STAMP(i) do { } while (0)
#define STAMP_FLUSH

DI float lo2f(u32 v) { return __uint_as_float(v << 16); }
DI float hi2f(u32 v) { return __uint_as_float(v & 0xffff0000u); }
DI u32 pk2(float lo, float hi) { f32x2 v = {lo, hi}; return __builtin_bit_cast(u32, __builtin_convertvector(v, bf16x2)); }
DI float rbf(float f) { return lo2f(pk2(f, 0.f)); }
DI rsrc_t mkrs(const void* p, u32 bytes) { return __builtin_amdgcn_make_buffer_rsrc((void*)p, 0, (int)bytes, 0x00020000); }
template <int AUX> DI u32x4 ldb(rsrc_t rs, u32 voff, u32 soff) { return __builtin_amdgcn_raw_buffer_load_b128(rs, voff, soff, AUX); }
DI void stb(u32x4 v, rsrc_t rs, u32 voff, u32 soff) { __builtin_amdgcn_raw_buffer_store_b128(v, rs, voff + soff, 0, AUX_SC1); }
DI void bar() { asm volatile("s_waitcnt lgkmcnt(0)\n\ts_barrier" ::: "memory"); }
DI void pin() { asm volatile("" ::: "memory"); }
DI f32x4 mf16(u32x4 a, u32x4 b, f32x4 c) { return __builtin_amdgcn_mfma_f32_16x16x32_bf16(__builtin_bit_cast(bf16x8, a), __builtin_bit_cast(bf16x8, b), c, 0, 0, 0); }
DI f32x16 mf32(u32x4 a, u32x4 b, f32x16 c) { return __builtin_amdgcn_mfma_f32_32x32x16_bf16(__builtin_bit_cast(bf16x8, a), __builtin_bit_cast(bf16x8, b), c, 0, 0, 0); }
DI float swap32max(float v) { auto r = __builtin_amdgcn_permlane32_swap(__float_as_uint(v), __float_as_uint(v), false, false); return fmaxf(__uint_as_float(r[0]), __uint_as_float(r[1])); }
DI float swap32sum(float v) { auto r = __builtin_amdgcn_permlane32_swap(__float_as_uint(v), __float_as_uint(v), false, false); return __uint_as_float(r[0]) + __uint_as_float(r[1]); }
DI float swap16max(float v) { auto r = __builtin_amdgcn_permlane16_swap(__float_as_uint(v), __float_as_uint(v), false, false); return fmaxf(__uint_as_float(r[0]), __uint_as_float(r[1])); }
DI float swap16sum(float v) { auto r = __builtin_amdgcn_permlane16_swap(__float_as_uint(v), __float_as_uint(v), false, false); return __uint_as_float(r[0]) + __uint_as_float(r[1]); }
template <int CTRL> DI float dpp(float v) { return __uint_as_float(__builtin_amdgcn_update_dpp(0, __float_as_uint(v), CTRL, 0xf, 0xf, true)); }
DI float wave_sum(float v) {
    v += dpp<0xb1>(v);
    v += dpp<0x4e>(v);
    v += dpp<0x141>(v);
    v += dpp<0x140>(v);
    v = swap16sum(v);
    return swap32sum(v);
}
DI void tr8(const u32x4 (&r)[8], u32x4 (&t)[8]) {
#pragma unroll
    for (int jj = 0; jj < 4; jj++) {
#pragma unroll
        for (int p = 0; p < 4; p++) {
            t[2 * jj][p] = __builtin_amdgcn_perm(r[2 * p + 1][jj], r[2 * p][jj], 0x05040100u);
            t[2 * jj + 1][p] = __builtin_amdgcn_perm(r[2 * p + 1][jj], r[2 * p][jj], 0x07060302u);
        }
    }
}
template <int AUX> DI void ld8(u32x4 (&r)[8], rsrc_t rs, u32 voff, u32 soff, u32 stride) {
#pragma unroll
    for (int i = 0; i < 8; i++) r[i] = ldb<AUX>(rs, voff, soff + i * stride);
}
DI void gemv8(const u32x4 (&r)[8], u32x4 a, f32x4 (&acc)[8]) {
    u32x4 t[8]; tr8(r, t);
#pragma unroll
    for (int j = 0; j < 8; j++) acc[j] = mf16(a, t[j], acc[j]);
}
DI u32 tag12(float v, u32 tg) { return ((__float_as_uint(v) + 0x800u) & 0xfffff000u) | tg; }
DI u32x4 lds128(const char* p) { return *(const u32x4*)p; }

#define XN_STRIDE 1216
#define H_XN   0
#define H_RED  5120
#define H_QB   (H_RED + 20480)
#define H_HB   (H_QB + 1280)
#define H_FIN  (H_HB + 640)
#define H_ST   (H_FIN + 2048)

DI void h_role(char* smem, const u16* x, const u16* wq, const u16* wuk, const u16* wg, const u16* wu, const u16* wd,
               u16* out, char* ws) {
    const int tid = threadIdx.x, l = tid & 63, w = __builtin_amdgcn_readfirstlane(tid >> 6);
    const int k = blockIdx.x, xc = k & 7, ii = k >> 3, h = 4 * xc + (ii >> 2), cq = ii & 3;
    const int n = l & 15, g = l >> 4;
    STAMP_DECL;
    STAMP(0);
    const rsrc_t rws = mkrs(ws, WS_END);
    const u32 E = __hip_atomic_load((const u32*)ws, __ATOMIC_RELAXED, __HIP_MEMORY_SCOPE_AGENT);
    u32x2 xv[3];
    {
        const rsrc_t rx = mkrs(x, 4 * 576 * 2);
        const u32 vo = (w < 4) ? (u32)(w * 1152 + l * 8) : OOB;
        xv[0] = __builtin_amdgcn_raw_buffer_load_b64(rx, vo, 0, 0);
        xv[1] = __builtin_amdgcn_raw_buffer_load_b64(rx, vo, 512, 0);
        xv[2] = __builtin_amdgcn_raw_buffer_load_b64(rx, l < 16 ? vo : OOB, 1024, 0);
    }
    const rsrc_t rq = mkrs(wq, 576 * 4096 * 2);
    const int rot = (9 * cq) >> 1;
    int b0 = w + rot; if (b0 >= 18) b0 -= 18;
    int b1 = w + 8 + rot; if (b1 >= 18) b1 -= 18;
    int b2 = w + 16 + rot; if (b2 >= 18) b2 -= 18;
    const u32 vq = (u32)(8 * g * 8192 + h * 256 + n * 16);
    u32x4 r0[8], r1[8], r2[8];
    u32x4 uk[4];
    ld8<AUX_WQ>(r0, rq, vq, b0 * 262144, 8192);
    {
        const rsrc_t ru = mkrs(wuk, 512 * 4096 * 2);
        const u32 vo = (u32)(((128 * cq + 16 * w + (n & 7)) * 32 + h) * 256 + ((n >> 3) * 4 + g) * 16);
#pragma unroll
        for (int s = 0; s < 4; s++) uk[s] = ldb<AUX_WUK>(ru, vo + (s & 1) * 128, (s >> 1) * 8 * 8192);
    }
    pin();
    if (w < 4) {
        float v[12];
#pragma unroll
        for (int i = 0; i < 3; i++) { v[4 * i] = lo2f(xv[i][0]); v[4 * i + 1] = hi2f(xv[i][0]); v[4 * i + 2] = lo2f(xv[i][1]); v[4 * i + 3] = hi2f(xv[i][1]); }
        float s = 0;
#pragma unroll
        for (int i = 0; i < 12; i++) s += v[i];
        const float mf = wave_sum(s) * (1.0f / 576);
        const float m = rbf(mf);
        float q = 0;
#pragma unroll
        for (int i = 0; i < 8; i++) { float d = v[i] - mf; q += d * d; }
        if (l < 16) {
#pragma unroll
            for (int i = 8; i < 12; i++) { float d = v[i] - mf; q += d * d; }
        }
        const float var = rbf(wave_sum(q) * (1.0f / 576));
        const float den = rbf(sqrtf(rbf(var + 1e-5f)));
#pragma unroll
        for (int i = 0; i < 3; i++) {
            u32x2 o;
            o[0] = pk2(rbf(v[4 * i] - m) / den, rbf(v[4 * i + 1] - m) / den);
            o[1] = pk2(rbf(v[4 * i + 2] - m) / den, rbf(v[4 * i + 3] - m) / den);
            if (i < 2 || l < 16) *(u32x2*)(smem + H_XN + w * XN_STRIDE + i * 512 + l * 8) = o;
        }
    }
    pin();
    STAMP(8);
    ld8<AUX_WQ>(r1, rq, vq, b1 * 262144, 8192);
    ld8<AUX_WQ>(r2, rq, w < 2 ? vq : OOB, b2 * 262144, 8192);
    STAMP(9);
    u32 clf = __builtin_amdgcn_raw_buffer_load_b32(rws, (u32)(k * 128), WS_CL, AUX_SC1);
    pin();
    bar();
    STAMP(1);
    {
        f32x4 acc[8];
#pragma unroll
        for (int j = 0; j < 8; j++) acc[j] = f32x4{0, 0, 0, 0};
        const char* xa = smem + H_XN + (n >> 2) * XN_STRIDE + g * 16;
        gemv8(r0, lds128(xa + b0 * 64), acc);
        gemv8(r1, lds128(xa + b1 * 64), acc);
        if (w < 2) gemv8(r2, lds128(xa + b2 * 64), acc);
        float* red = (float*)(smem + H_RED) + (w * 4 + g) * 128 + 4 * n;
        *(f32x4*)red = f32x4{acc[0][0], acc[1][0], acc[2][0], acc[3][0]};
        *(f32x4*)(red + 64) = f32x4{acc[4][0], acc[5][0], acc[6][0], acc[7][0]};
    }
    STAMP(2);
    bar();
    {
        const float* red = (const float*)(smem + H_RED) + tid;
        float s = 0;
#pragma unroll
        for (int i = 0; i < 8; i++) s += red[i * 512];
        const int b = tid >> 7, idx = tid & 127, d = 8 * ((idx >> 2) & 15) + 4 * (idx >> 6) + (idx & 3);
        *(u16*)(smem + H_QB + b * 320 + d * 2) = (u16)pk2(s, 0.f);
    }
    bar();
    const u32 T = E + 1;
    {
        const char* qa = smem + H_QB + (n & 3) * 320 + ((n >> 2) & 1) * 64 + g * 16;
        const u32x4 q0 = lds128(qa), q1 = lds128(qa + 128);
        f32x4 a0 = mf16(uk[0], q0, f32x4{0, 0, 0, 0}), a1 = mf16(uk[2], q0, f32x4{0, 0, 0, 0});
        a0 = mf16(uk[1], q1, a0); a1 = mf16(uk[3], q1, a1);
        if (n < 8 && (n >> 2) == (g >> 1)) {
            float* st = (float*)(smem + H_ST) + (g >> 1) * 512 + (n & 3) * 128 + 16 * w + 4 * (g & 1);
            *(f32x4*)st = a0; *(f32x4*)(st + 8) = a1;
        }
        if (w < 2) {
            for (int it = 0; it < SPIN_MAX && clf != T; it++) { __builtin_amdgcn_s_sleep(1); clf = __builtin_amdgcn_raw_buffer_load_b32(rws, (u32)(k * 128), WS_CL, AUX_SC1); }
        }
        bar();
        if (w < 2) {
            const float* st = (const float*)(smem + H_ST) + tid * 4;
            const f32x4 v = *(const f32x4*)st + *(const f32x4*)(st + 512);
            const float sc = 0.08838834764831845f * 1.4426950408889634f;
            const u32 tg = T & 0xffffu;
            u32x4 o;
#pragma unroll
            for (int r = 0; r < 4; r++) o[r] = (pk2(0.f, v[r] * sc) & 0xffff0000u) | tg;
            stb(o, rws, (u32)((((tid >> 5) * 4 + cq) * 32 + h) * 512 + (tid & 31) * 16), WS_QA);
        }
    }
    STAMP(3);
    const u32 tg12 = T & 0xfffu;
    if (k >= 56) {
        const int u = k - 56;
        const bool gu3 = (w & 3) == 0, dn = !gu3 && w != 7;
        const int t = w < 4 ? w - 1 : w - 2;
        const rsrc_t rg = mkrs(w < 4 ? wg : wu, 576 * 4608 * 2);
        const rsrc_t rd = mkrs(gu3 ? (w < 4 ? wg : wu) : wd, 576 * 4608 * 2);
        u32x4 f0[8], f1[8], f2[8], f3[8];
        const u32 vg = (u32)(((n >> 3) * 32 + 8 * g) * 9216 + (n & 7) * 16 + u * 128);
        const u32 vd = (!dn || (t == 4 && n >= 8)) ? OOB : (u32)(8 * g * 1152 + n * 16 + t * 256);
        ld8<AUX_FFN>(f0, rg, vg, (w & 3) * 128 * 9216, 9216);
        ld8<AUX_FFN>(f1, rg, vg, ((w & 3) * 128 + 64) * 9216, 9216);
        ld8<AUX_FFN>(f2, rd, gu3 ? vg : vd, gu3 ? 512 * 9216 : u * 64 * 1152, gu3 ? 9216 : 1152);
        ld8<AUX_FFN>(f3, rd, vd, (u * 64 + 32) * 1152, 1152);
        pin();
        STAMP(10);
        const char* xa = smem + H_XN + (n & 3) * XN_STRIDE + (((n >> 2) & 1) * 32 + 8 * g) * 2;
        float* red = (float*)(smem + H_RED);
        {
            f32x4 acc[8];
#pragma unroll
            for (int j = 0; j < 8; j++) acc[j] = f32x4{0, 0, 0, 0};
            gemv8(f0, lds128(xa + (w & 3) * 256), acc);
            gemv8(f1, lds128(xa + (w & 3) * 256 + 128), acc);
            if (gu3) gemv8(f2, lds128(xa + 1024), acc);
            if ((g & 1) == (n >> 3)) {
                float* p = red + ((w * 2 + (n >> 3)) * 4 + (g >> 1) * 2) * 64 + (n & 7) * 8;
                const bool up = g >= 2;
#pragma unroll
                for (int b = 0; b < 2; b++) {
                    *(f32x4*)(p + b * 64) = up ? f32x4{acc[0][2 + b], acc[1][2 + b], acc[2][2 + b], acc[3][2 + b]} : f32x4{acc[0][b], acc[1][b], acc[2][b], acc[3][b]};
                    *(f32x4*)(p + b * 64 + 4) = up ? f32x4{acc[4][2 + b], acc[5][2 + b], acc[6][2 + b], acc[7][2 + b]} : f32x4{acc[4][b], acc[5][b], acc[6][b], acc[7][b]};
                }
            }
        }
        bar();
        STAMP(11);
        if (tid < 256) {
            float gs = 0, us = 0;
#pragma unroll
            for (int i = 0; i < 8; i++) { gs += red[i * 256 + tid]; us += red[(8 + i) * 256 + tid]; }
            const float gg = rbf(gs), uu = rbf(us);
            const float inner = 0.7978845608028654f * (gg + 0.044715f * gg * gg * gg);
            const float ge = rbf(0.5f * gg * (1.0f + tanhf(inner)));
            *(u16*)(smem + H_HB + (tid >> 6) * 144 + (tid & 63) * 2) = (u16)pk2(ge * uu, 0.f);
        }
        bar();
        if (dn) {
            f32x4 acc[8];
#pragma unroll
            for (int j = 0; j < 8; j++) acc[j] = f32x4{0, 0, 0, 0};
            const char* ha = smem + H_HB + (n >> 2) * 144 + g * 16;
            gemv8(f2, lds128(ha), acc);
            gemv8(f3, lds128(ha + 64), acc);
            if (t < 4 || n < 8) {
                const bool up = (n & 4) != 0;
                u32x4 va, vb;
#pragma unroll
                for (int e = 0; e < 4; e++) {
                    const u32 lo = tag12(acc[e][0], tg12), hi = tag12(acc[4 + e][0], tg12);
                    const u32 fr = __builtin_amdgcn_update_dpp(0, hi, 0x114, 0xf, 0xf, true);
                    const u32 fl = __builtin_amdgcn_update_dpp(0, hi, 0x104, 0xf, 0xf, true);
                    va[e] = up ? fr : lo; vb[e] = up ? lo : fl;
                }
                const u32 vo = (u32)(((g * 18 + 4 * t + 2 * (n >> 3)) * 72 + u) * 128 + (n & 3) * 32);
                stb(va, rws, vo + (up ? 16 : 0), WS_YP);
                stb(vb, rws, vo + 72 * 128 + (up ? 0 : 16), WS_YP);
            }
        }
    }
    STAMP(4);
    if (k >= 56) {
        const int r = k - 56, b = r / 18, m0 = (r - b * 18) * 32;
        float xres = 0.f;
        if (tid < 32) xres = lo2f(x[b * 576 + m0 + tid]);
        f32x4 ys = {0, 0, 0, 0}, ss = {0, 0, 0, 0};
        {
            const u32 vo = (u32)(r * 72 * 128 + tid * 16);
            for (int it = 0; it < SPIN_MAX; it++) {
                u32x4 a = ldb<AUX_SC1>(rws, vo, WS_YP);
                u32x4 d = ldb<AUX_SC1>(rws, tid < 64 ? vo + 8192 : OOB, WS_YP);
                u32 bad = 0;
#pragma unroll
                for (int e = 0; e < 4; e++) bad |= (a[e] ^ tg12) | (tid < 64 ? (d[e] ^ tg12) : 0u);
                if (__all((bad & 0xfffu) == 0) || it == SPIN_MAX - 1) {
#pragma unroll
                    for (int e = 0; e < 4; e++) ys[e] = __uint_as_float(a[e] & 0xfffff000u) + __uint_as_float(d[e] & 0xfffff000u);
                    break;
                }
                __builtin_amdgcn_s_sleep(4);
            }
        }
        STAMP(5);
        {
            const u32 vo = (u32)(r * 128 * 128 + tid * 16);
            for (int it = 0; it < SPIN_MAX; it++) {
                u32x4 a = ldb<AUX_SC1>(rws, vo, WS_SP), c = ldb<AUX_SC1>(rws, vo + 8192, WS_SP);
                u32 bad = 0;
#pragma unroll
                for (int e = 0; e < 4; e++) bad |= (a[e] ^ tg12) | (c[e] ^ tg12);
                if (__all((bad & 0xfffu) == 0) || it == SPIN_MAX - 1) {
#pragma unroll
                    for (int e = 0; e < 4; e++) ss[e] = __uint_as_float(a[e] & 0xfffff000u) + __uint_as_float(c[e] & 0xfffff000u);
                    break;
                }
                __builtin_amdgcn_s_sleep(1);
            }
        }
        STAMP(6);
#pragma unroll
        for (int e = 0; e < 4; e++) {
            ys[e] += dpp<0x128>(ys[e]); ss[e] += dpp<0x128>(ss[e]);
            ys[e] = swap32sum(swap16sum(ys[e])); ss[e] = swap32sum(swap16sum(ss[e]));
        }
        float* fin = (float*)(smem + H_FIN);
        if (l < 8) { *(f32x4*)(fin + w * 32 + l * 4) = ys; *(f32x4*)(fin + 256 + w * 32 + l * 4) = ss; }
        bar();
        if (tid < 32) {
            float y = 0, s = 0;
#pragma unroll
            for (int i = 0; i < 8; i++) { y += fin[i * 32 + tid]; s += fin[256 + i * 32 + tid]; }
            y = rbf(y); s = rbf(s);
            const float gt = rbf(y / (1.0f + __expf(-y)));
            out[b * 576 + m0 + tid] = (u16)pk2(xres + rbf(s * gt), 0.f);
        }
        if (k == 56 && tid == 0) __hip_atomic_store((u32*)ws, T, __ATOMIC_RELAXED, __HIP_MEMORY_SCOPE_AGENT);
        STAMP(7);
    }
    STAMP_FLUSH;
}

#define A_Q 131072
DI void dma_row(const u16* gbase, u32 voff, u32 ldsaddr) {
    u32 keep;
    asm volatile("s_mov_b32 %0, m0\n\ts_mov_b32 m0, %3\n\ts_nop 0\n\tglobal_load_lds_dwordx4 %1, %2" KV_NT "\n\ts_mov_b32 m0, %0"
                 : "=&s"(keep) : "v"(voff), "s"(gbase), "s"(ldsaddr) : "memory");
}
template <int OFF> DI void ld_sc1_asm(u32x4& r, u32 voff, const char* base) {
    asm volatile("global_load_dwordx4 %0, %1, %2 offset:%3 sc1" : "=&v"(r) : "v"(voff), "s"(base), "n"(OFF) : "memory");
}

DI void a_weights(u32x4 (&w0)[8], u32x4 (&w1)[8], u32x4 (&w2)[8], const u16* wuv, const u16* wo, int w, int h, int al, int n, int g) {
    const int kb = w & 3;
    const rsrc_t ro = mkrs(wo, 4096 * 576 * 2);
    const u32 vo = (u32)(8 * g * 1152 + n * 16);
    const u32 so = (u32)((h * 128 + 32 * kb) * 1152);
    if (w < 4) {
        const rsrc_t rv = mkrs(wuv, 512 * 4096 * 2);
        ld8<AUX_WUV>(w0, rv, (u32)(8 * g * 8192 + h * 256 + n * 16), (128 * al + 32 * kb) * 8192, 8192);
    } else {
        ld8<AUX_WO>(w0, ro, vo, so, 1152);
    }
    ld8<AUX_WO>(w1, ro, vo, so + (1 + (w >> 2)) * 256, 1152);
    ld8<AUX_WO>(w2, ro, (w >= 4 && n >= 8) ? OOB : vo, so + (3 + (w >> 2)) * 256, 1152);
}

DI void a_role(char* smem, const u16* ckv, const u16* wuv, const u16* wo, char* ws) {
    const int tid = threadIdx.x, l = tid & 63, w = __builtin_amdgcn_readfirstlane(tid >> 6);
    const int a = blockIdx.x - 128, xc = a & 7, ii = a >> 3;
    const int b = xc >> 1, chunk = (xc & 1) * 16 + ii;
    const int h = 4 * xc + (ii >> 2), al = ii & 3;
    const int n = l & 15, g = l >> 4, hh = l >> 5;
    STAMP_DECL;
    STAMP(0);
    const rsrc_t rws = mkrs(ws, WS_END);
    u32 E;
    asm volatile("global_load_dword %0, %1, %2 sc1" : "=&v"(E) : "v"(0u), "s"(ws) : "memory");
    {
        const u32x4 ones = {~0u, ~0u, ~0u, ~0u};
#pragma unroll
        for (int i = 0; i < 4; i++) stb(ones, rws, (u32)(tid * 16 + i * 8192), WS_PART + (h * 4 + al) * 32768);
        stb(ones, rws, tid < 16 ? (u32)(tid * 16 + a * 256 + WS_ST) : OOB, 0);
    }
    const u32 lds0 = (u32)(size_t)(LAS char*)smem;
    const u16* kb0 = ckv + ((size_t)b * 4096 + chunk * 128 + 16 * w) * 512;
    const u32 ldsw = lds0 + w * 16384;
#pragma unroll
    for (int i = 0; i < CLR_EARLY; i++) dma_row(kb0, (u32)(((l ^ (((i & 3) << 2) | (i >> 2))) << 4) + i * 1024), ldsw + i * 1024);
    asm volatile("s_waitcnt vmcnt(%1)" : "+v"(E) : "n"(CLR_EARLY) : "memory");
    const u32 T = E + 1;
    bar();
    if (tid == 0) __builtin_amdgcn_raw_buffer_store_b32(T, rws, (u32)(a * 128 + WS_CL), 0, AUX_SC1);
#pragma unroll
    for (int i = CLR_EARLY; i < 16; i++) dma_row(kb0, (u32)(((l ^ (((i & 3) << 2) | (i >> 2))) << 4) + i * 1024), ldsw + i * 1024);
    u32x4 w0[8], w1[8], w2[8];
    if (w < 4) a_weights(w0, w1, w2, wuv, wo, w, h, al, n, g);
    asm volatile("s_waitcnt vmcnt(0)" ::: "memory");
    {
        const char* qsrc = ws + WS_QA + b * 65536 + w * 8192;
        const u32 vq = l * 16 + 4096;
        const u32 tg = T & 0xffffu;
        u32x4 q[8];
        STAMP(1);
        for (int it = 0; it < SPIN_MAX; it++) {
            ld_sc1_asm<-4096>(q[0], vq, qsrc); ld_sc1_asm<-3072>(q[1], vq, qsrc);
            ld_sc1_asm<-2048>(q[2], vq, qsrc); ld_sc1_asm<-1024>(q[3], vq, qsrc);
            ld_sc1_asm<0>(q[4], vq, qsrc); ld_sc1_asm<1024>(q[5], vq, qsrc);
            ld_sc1_asm<2048>(q[6], vq, qsrc); ld_sc1_asm<3072>(q[7], vq, qsrc);
            asm volatile("s_waitcnt vmcnt(0)" : "+v"(q[0]), "+v"(q[1]), "+v"(q[2]), "+v"(q[3]), "+v"(q[4]), "+v"(q[5]), "+v"(q[6]), "+v"(q[7]) :: "memory");
            u32 bad = 0;
#pragma unroll
            for (int i = 0; i < 8; i++) bad |= (q[i][0] ^ tg) | (q[i][1] ^ tg) | (q[i][2] ^ tg) | (q[i][3] ^ tg);
            if (__all((bad & 0xffffu) == 0)) break;
            __builtin_amdgcn_s_sleep(POLL_SLEEP);
        }
        STAMP(2);
        const int z = (l >> 1) & 7;
        char* qd = smem + A_Q + (8 * (w >> 1) + ((l & 31) >> 2)) * 1024 + ((l >> 1) & 1) * 512 + (l & 1) * 8;
#pragma unroll
        for (int i = 0; i < 8; i++) {
            const int hq = 16 * (w & 1) + 2 * i + hh;
            u32x2 o = {__builtin_amdgcn_perm(q[i][1], q[i][0], 0x07060302u), __builtin_amdgcn_perm(q[i][3], q[i][2], 0x07060302u)};
            *(u32x2*)(qd + ((hq ^ z) << 4)) = o;
        }
    }
    bar();
    STAMP(3);
    if (w >= 4) { a_weights(w0, w1, w2, wuv, wo, w, h, al, n, g); pin(); }
    f32x16 sacc;
    float mx = 0.f;
    if (w < 4) {
#pragma unroll
        for (int r = 0; r < 16; r++) sacc[r] = 0.f;
        const int row = 32 * w + (l & 31);
        const int f = ((row & 3) << 2) | ((row >> 2) & 3);
        const char* kp = smem + row * 1024;
        const int fx = (f ^ hh) << 4;
        const char* qp = smem + A_Q + hh * 512;
#pragma unroll
        for (int s = 0; s < 32; s++) {
            const u32x4 ka = lds128(kp + ((32 * (s & 7)) ^ fx) + 256 * (s >> 3));
            const u32x4 qb = lds128(qp + s * 1024 + (((l & 31) ^ ((s & 3) * 2 + hh)) << 4));
            sacc = mf32(ka, qb, sacc);
        }
        mx = sacc[0];
#pragma unroll
        for (int r = 1; r < 16; r++) mx = fmaxf(mx, sacc[r]);
        mx = swap32max(mx);
    }
    bar();
    float* aux = (float*)(smem + A_Q);
    if (w < 4 && l < 32) aux[w * 32 + l] = mx;
    bar();
    if (w < 4) {
        const int hq = l & 31;
        const float M = fmaxf(fmaxf(aux[hq], aux[32 + hq]), fmaxf(aux[64 + hq], aux[96 + hq]));
        float p[16], ls = 0.f;
#pragma unroll
        for (int r = 0; r < 16; r++) { p[r] = __builtin_amdgcn_exp2f(sacc[r] - M); ls += p[r]; }
        ls = swap32sum(ls);
        if (l < 32) aux[128 + w * 32 + l] = ls;
        char* pd = smem + A_Q + 1024 + (2 * w) * 1024 + l * 16;
        *(u32x4*)pd = u32x4{pk2(p[0], p[1]), pk2(p[2], p[3]), pk2(p[4], p[5]), pk2(p[6], p[7])};
        *(u32x4*)(pd + 1024) = u32x4{pk2(p[8], p[9]), pk2(p[10], p[11]), pk2(p[12], p[13]), pk2(p[14], p[15])};
    }
    bar();
    STAMP(4);
    if (w == 4 && l < 16) {
        u32x4 o;
#pragma unroll
        for (int e = 0; e < 2; e++) {
            const int hq = 2 * l + e;
            const float M = fmaxf(fmaxf(aux[hq], aux[32 + hq]), fmaxf(aux[64 + hq], aux[96 + hq]));
            const float L = (aux[128 + hq] + aux[160 + hq]) + (aux[192 + hq] + aux[224 + hq]);
            o[2 * e] = min(__float_as_uint(M), 0xfffffffeu); o[2 * e + 1] = min(__float_as_uint(L), 0xfffffffeu);
        }
        stb(o, rws, (u32)((b * 32 + chunk) * 256 + l * 16), WS_ST);
    }
    {
        f32x16 o0, o1;
#pragma unroll
        for (int r = 0; r < 16; r++) { o0[r] = 0.f; o1[r] = 0.f; }
        const int q4 = (l & 15) >> 2, p4 = l & 3, blk = (l >> 4) & 1;
        const char* vb = smem + (4 * hh + q4) * 1024 + (w >> 1) * 256 + (p4 >> 1) * 8;
        const int cl = 8 * (w & 1) + 2 * blk + (p4 & 1);
        const char* pp = smem + A_Q + 1024 + l * 16;
#pragma unroll
        for (int ks = 0; ks < 8; ks++) {
            const u32x4 pb = lds128(pp + ks * 1024);
            u32x4 va[2];
#pragma unroll
            for (int cb = 0; cb < 2; cb++) {
#pragma unroll
                for (int t = 0; t < 2; t++) {
                    const int f = (q4 << 2) | (hh + 2 * t);
                    const char* ad = vb + ks * 16384 + t * 8192 + (((cl + 4 * cb) ^ f) << 4);
                    s16x4 v = __builtin_amdgcn_ds_read_tr16_b64_v4i16((LAS s16x4*)ad);
                    u32x2 vv = __builtin_bit_cast(u32x2, v);
                    va[cb][2 * t] = vv[0]; va[cb][2 * t + 1] = vv[1];
                }
            }
            o0 = mf32(va[0], pb, o0);
            o1 = mf32(va[1], pb, o1);
        }
        STAMP(5);
        u32x4 s0[2], s1[2];
#pragma unroll
        for (int i = 0; i < 2; i++) {
#pragma unroll
            for (int e = 0; e < 4; e++) {
                s0[i][e] = min(pk2(o0[8 * i + 2 * e], o0[8 * i + 2 * e + 1]), 0xfffffffeu);
                s1[i][e] = min(pk2(o1[8 * i + 2 * e], o1[8 * i + 2 * e + 1]), 0xfffffffeu);
            }
        }
        char* stg = smem + A_Q + 9216 + w * 2304;
        char* sw = stg + (l & 15) * 144 + hh * 16;
        const char* sr = stg + (l >> 3) * 144 + (l & 7) * 16;
        const u32 vo = (u32)((((l >> 3) * 4 + (w >> 1)) * 128 + b * 32 + chunk) * 256 + (w & 1) * 128 + (l & 7) * 16);
#pragma unroll
        for (int rd = 0; rd < 2; rd++) {
            if (((l >> 4) & 1) == rd) {
                *(u32x4*)(sw) = s0[0]; *(u32x4*)(sw + 32) = s0[1]; *(u32x4*)(sw + 64) = s1[0]; *(u32x4*)(sw + 96) = s1[1];
            }
            asm volatile("" ::: "memory");
            const u32x4 v0 = lds128(sr), v1 = lds128(sr + 8 * 144);
            stb(v0, rws, vo + (16 * rd) * 131072, WS_PART);
            stb(v1, rws, vo + (16 * rd + 8) * 131072, WS_PART);
            asm volatile("" ::: "memory");
        }
    }
    STAMP(6);
    u32x4 t0[8], t1[8], t2[8];
    tr8(w0, t0); tr8(w1, t1); tr8(w2, t2);
#pragma unroll
    for (int j = 0; j < 8; j++) asm volatile("" : "+v"(t0[j]), "+v"(t1[j]), "+v"(t2[j]));
    STAMP(7);
#define D_WT (A_Q + 28672)
#define D_OB 4096
#define D_CX 8192
#define D_SR 16384
    u32x4 pt[8];
    if (w < 4) {
        const u32 vo = (u32)((w * 32 + 8 * g) * 256 + n * 16);
        const u32 so = WS_PART + (h * 4 + al) * 32768;
        u32x4 pr[8];
        for (int it = 0; it < SPIN_MAX; it++) {
#pragma unroll
            for (int i = 0; i < 8; i++) pr[i] = ldb<AUX_SC1>(rws, vo + i * 256, so);
            u32 mxv = 0;
#pragma unroll
            for (int i = 0; i < 8; i++) mxv = max(max(mxv, max(pr[i][0], pr[i][1])), max(pr[i][2], pr[i][3]));
            if (__all(mxv != 0xffffffffu)) break;
            __builtin_amdgcn_s_sleep(1);
        }
        tr8(pr, pt);
    }
    if (w == 4 || w == 5) {
        const u32 vo = (u32)(((2 * (w - 4) + hh) * 32 + (l & 31)) * 256 + h * 8);
        u32x2 sr;
        for (int it = 0; it < SPIN_MAX; it++) {
            sr = __builtin_amdgcn_raw_buffer_load_b64(rws, vo, WS_ST, AUX_SC1);
            if (__all(max(sr[0], sr[1]) != 0xffffffffu)) break;
            __builtin_amdgcn_s_sleep(1);
        }
        const float m = __uint_as_float(sr[0]);
        float M = m;
        M = fmaxf(M, dpp<0xb1>(M)); M = fmaxf(M, dpp<0x4e>(M)); M = fmaxf(M, dpp<0x141>(M)); M = fmaxf(M, dpp<0x140>(M)); M = swap16max(M);
        const float e = __builtin_amdgcn_exp2f(m - M);
        float L = e * __uint_as_float(sr[1]);
        L += dpp<0xb1>(L); L += dpp<0x4e>(L); L += dpp<0x141>(L); L += dpp<0x140>(L); L = swap16sum(L);
        const float a = e / L;
        const u32 hi = pk2(a, 0.f);
        const u32 lo = pk2(a - lo2f(hi), 0.f);
        u16* wt = (u16*)(smem + D_WT) + (2 * (w - 4) + hh) * 64 + (l & 31);
        wt[0] = (u16)hi; wt[32] = (u16)lo;
    }
    bar();
    STAMP(8);
    if (w < 4) {
        u32x4 aw = lds128(smem + D_WT + w * 128 + (n & 1) * 64 + g * 16);
        if (n >= 2) aw = u32x4{0, 0, 0, 0};
        float o[8];
#pragma unroll
        for (int j = 0; j < 8; j++) { f32x4 d = mf16(aw, pt[j], f32x4{0, 0, 0, 0}); o[j] = d[0] + d[1]; }
        if (g == 0) *(u32x4*)(smem + D_OB + w * 320 + n * 16) = u32x4{pk2(o[0], o[1]), pk2(o[2], o[3]), pk2(o[4], o[5]), pk2(o[6], o[7])};
    }
    bar();
    if (w < 4) {
        const u32x4 ao = lds128(smem + D_OB + (n >> 2) * 320 + (32 * w + 8 * g) * 2);
        f32x4 d[8];
#pragma unroll
        for (int j = 0; j < 8; j++) d[j] = mf16(ao, t0[j], f32x4{0, 0, 0, 0});
        float* cx = (float*)(smem + D_CX) + (w * 4 + g) * 128 + 8 * n;
        *(f32x4*)cx = f32x4{d[0][0], d[1][0], d[2][0], d[3][0]};
        *(f32x4*)(cx + 4) = f32x4{d[4][0], d[5][0], d[6][0], d[7][0]};
    }
    bar();
    {
        const int kb = w & 3;
        const float* cx = (const float*)(smem + D_CX) + (n >> 2) * 128 + 32 * kb + 8 * g;
        f32x4 lo = *(const f32x4*)cx, hi = *(const f32x4*)(cx + 4);
#pragma unroll
        for (int i = 1; i < 4; i++) { lo += *(const f32x4*)(cx + i * 512); hi += *(const f32x4*)(cx + i * 512 + 4); }
        const u32x4 ac = {pk2(lo[0], lo[1]), pk2(lo[2], lo[3]), pk2(hi[0], hi[1]), pk2(hi[2], hi[3])};
        float* sr = (float*)(smem + D_SR) + (kb * 4 + g) * 640 + 8 * n;
        f32x4 d[8];
        if (w >= 4) {
#pragma unroll
            for (int j = 0; j < 8; j++) d[j] = mf16(ac, t0[j], f32x4{0, 0, 0, 0});
            *(f32x4*)sr = f32x4{d[0][0], d[1][0], d[2][0], d[3][0]};
            *(f32x4*)(sr + 4) = f32x4{d[4][0], d[5][0], d[6][0], d[7][0]};
        }
#pragma unroll
        for (int j = 0; j < 8; j++) d[j] = mf16(ac, t1[j], f32x4{0, 0, 0, 0});
        float* s1 = sr + (1 + (w >> 2)) * 128;
        *(f32x4*)s1 = f32x4{d[0][0], d[1][0], d[2][0], d[3][0]};
        *(f32x4*)(s1 + 4) = f32x4{d[4][0], d[5][0], d[6][0], d[7][0]};
#pragma unroll
        for (int j = 0; j < 8; j++) d[j] = mf16(ac, t2[j], f32x4{0, 0, 0, 0});
        float* s2 = sr + (3 + (w >> 2)) * 128;
        *(f32x4*)s2 = f32x4{d[0][0], d[1][0], d[2][0], d[3][0]};
        *(f32x4*)(s2 + 4) = f32x4{d[4][0], d[5][0], d[6][0], d[7][0]};
    }
    bar();
    {
        const u32 tg12 = T & 0xfffu;
#pragma unroll
        for (int i = 0; i < 2; i++) {
            const int ch = tid + 512 * i;
            if (ch < 576) {
                const int bb = ch / 144, m = (ch - bb * 144) * 4;
                const float* sr = (const float*)(smem + D_SR) + bb * 640 + m;
                f32x4 s = *(const f32x4*)sr;
#pragma unroll
                for (int q = 1; q < 4; q++) s += *(const f32x4*)(sr + q * 2560);
                stb(u32x4{tag12(s[0], tg12), tag12(s[1], tg12), tag12(s[2], tg12), tag12(s[3], tg12)}, rws,
                    (u32)(((bb * 18 + (m >> 5)) * 128 + a) * 128 + (m & 31) * 4), WS_SP);
            }
        }
    }
    STAMP(9);
    STAMP_FLUSH;
}

__global__ __launch_bounds__(512, 2) void fused_decode(const u16* x, const u16* ckv, const u16* wq, const u16* wuk, const u16* wuv,
                                                       const u16* wo, const u16* wg, const u16* wu, const u16* wd, u16* out, char* ws) {
    extern __shared__ __attribute__((aligned(16))) char smem[];
    if (blockIdx.x < 128) h_role(smem, x, wq, wuk, wg, wu, wd, out, ws);
    else a_role(smem, ckv, wuv, wo, ws);
}

extern "C" void kernel_launch(void* const* d_in, const int* in_sizes, int n_in, void* d_out, int out_size, void* d_ws, size_t ws_size,
                              hipStream_t stream) {
    (void)in_sizes; (void)n_in; (void)out_size; (void)ws_size;
    hipFuncSetAttribute((const void*)fused_decode, hipFuncAttributeMaxDynamicSharedMemorySize, 163840);
    fused_decode<<<256, 512, 163840, stream>>>((const u16*)d_in[0], (const u16*)d_in[1], (const u16*)d_in[2], (const u16*)d_in[3],
                                               (const u16*)d_in[4], (const u16*)d_in[5], (const u16*)d_in[7], (const u16*)d_in[8],
                                               (const u16*)d_in[9], (u16*)d_out, (char*)d_ws);
}
```

```cpp
#include <hip/hip_runtime.h>
typedef unsigned short u16;
typedef unsigned int u32;
typedef unsigned long long u64;
typedef u32 u32x4 __attribute__((ext_vector_type(4)));
typedef u32 u32x2 __attribute__((ext_vector_type(2)));
typedef float f32x4 __attribute__((ext_vector_type(4)));
typedef float f32x2 __attribute__((ext_vector_type(2)));
typedef float f32x16 __attribute__((ext_vector_type(16)));
typedef __bf16 bf16x8 __attribute__((ext_vector_type(8)));
typedef __bf16 bf16x2 __attribute__((ext_vector_type(2)));
typedef short s16x4 __attribute__((ext_vector_type(4)));
typedef __amdgpu_buffer_rsrc_t rsrc_t;
#define DI __device__ __forceinline__
#define LAS __attribute__((address_space(3)))

#define WS_QA   4096u
#define WS_PART (WS_QA + 262144u)
#define WS_ST   (WS_PART + 4194304u)
#define WS_SP   (WS_ST + 32768u)
#define WS_YP   (WS_SP + 1179648u)
#define WS_CL   (WS_YP + 1327104u)
#define WS_END  (WS_CL + 16384u)
#define WS_DBG  (8u << 20)

#define SPIN_MAX 20000
#define AUX_WQ 0
#define AUX_WUK 0
#define AUX_FFN 2
#define AUX_WUV 2
#define AUX_WO 0
#define KV_NT " nt"
#define DMA_SPLIT 12
#define DMA_WAIT 4
#define ZPRIME 1
#define FFN_DELAY 0
#define A_DELAY 0
#define CLR_EARLY 8
#define POLL_SLEEP 1
#define HORD 1
#define AUX_SC1 16
#define OOB 0x80000000u

#define STAMP_DECL
#define STAMP(i) do { } while (0)
#define STAMP_FLUSH

DI float lo2f(u32 v) { return __uint_as_float(v << 16); }
DI float hi2f(u32 v) { return __uint_as_float(v & 0xffff0000u); }
DI u32 pk2(float lo, float hi) { f32x2 v = {lo, hi}; return __builtin_bit_cast(u32, __builtin_convertvector(v, bf16x2)); }
DI float rbf(float f) { return lo2f(pk2(f, 0.f)); }
DI rsrc_t mkrs(const void* p, u32 bytes) { return __builtin_amdgcn_make_buffer_rsrc((void*)p, 0, (int)bytes, 0x00020000); }
template <int AUX> DI u32x4 ldb(rsrc_t rs, u32 voff, u32 soff) { return __builtin_amdgcn_raw_buffer_load_b128(rs, voff, soff, AUX); }
DI void stb(u32x4 v, rsrc_t rs, u32 voff, u32 soff) { __builtin_amdgcn_raw_buffer_store_b128(v, rs, voff + soff, 0, AUX_SC1); }
DI void bar() { asm volatile("s_waitcnt lgkmcnt(0)\n\ts_barrier" ::: "memory"); }
DI void pin() { asm volatile("" ::: "memory"); }
DI f32x4 mf16(u32x4 a, u32x4 b, f32x4 c) { return __builtin_amdgcn_mfma_f32_16x16x32_bf16(__builtin_bit_cast(bf16x8, a), __builtin_bit_cast(bf16x8, b), c, 0, 0, 0); }
DI f32x16 mf32(u32x4 a, u32x4 b, f32x16 c) { return __builtin_amdgcn_mfma_f32_32x32x16_bf16(__builtin_bit_cast(bf16x8, a), __builtin_bit_cast(bf16x8, b), c, 0, 0, 0); }
DI float swap32max(float v) { auto r = __builtin_amdgcn_permlane32_swap(__float_as_uint(v), __float_as_uint(v), false, false); return fmaxf(__uint_as_float(r[0]), __uint_as_float(r[1])); }
DI float swap32sum(float v) { auto r = __builtin_amdgcn_permlane32_swap(__float_as_uint(v), __float_as_uint(v), false, false); return __uint_as_float(r[0]) + __uint_as_float(r[1]); }
DI float swap16max(float v) { auto r = __builtin_amdgcn_permlane16_swap(__float_as_uint(v), __float_as_uint(v), false, false); return fmaxf(__uint_as_float(r[0]), __uint_as_float(r[1])); }
DI float swap16sum(float v) { auto r = __builtin_amdgcn_permlane16_swap(__float_as_uint(v), __float_as_uint(v), false, false); return __uint_as_float(r[0]) + __uint_as_float(r[1]); }
template <int CTRL> DI float dpp(float v) { return __uint_as_float(__builtin_amdgcn_update_dpp(0, __float_as_uint(v), CTRL, 0xf, 0xf, true)); }
DI float wave_sum(float v) {
    v += dpp<0xb1>(v);
    v += dpp<0x4e>(v);
    v += dpp<0x141>(v);
    v += dpp<0x140>(v);
    v = swap16sum(v);
    return swap32sum(v);
}
DI void tr8(const u32x4 (&r)[8], u32x4 (&t)[8]) {
#pragma unroll
    for (int jj = 0; jj < 4; jj++) {
#pragma unroll
        for (int p = 0; p < 4; p++) {
            t[2 * jj][p] = __builtin_amdgcn_perm(r[2 * p + 1][jj], r[2 * p][jj], 0x05040100u);
            t[2 * jj + 1][p] = __builtin_amdgcn_perm(r[2 * p + 1][jj], r[2 * p][jj], 0x07060302u);
        }
    }
}
template <int AUX> DI void ld8(u32x4 (&r)[8], rsrc_t rs, u32 voff, u32 soff, u32 stride) {
#pragma unroll
    for (int i = 0; i < 8; i++) r[i] = ldb<AUX>(rs, voff, soff + i * stride);
}
DI void gemv8(const u32x4 (&r)[8], u32x4 a, f32x4 (&acc)[8]) {
    u32x4 t[8]; tr8(r, t);
#pragma unroll
    for (int j = 0; j < 8; j++) acc[j] = mf16(a, t[j], acc[j]);
}
DI u32 tag12(float v, u32 tg) { return ((__float_as_uint(v) + 0x800u) & 0xfffff000u) | tg; }
DI u32x4 lds128(const char* p) { return *(const u32x4*)p; }

#define XN_STRIDE 1216
#define H_XN   0
#define H_RED  5120
#define H_QB   (H_RED + 20480)
#define H_HB   (H_QB + 1280)
#define H_FIN  (H_HB + 640)
#define H_ST   (H_FIN + 2048)

DI void h_role(char* smem, const u16* x, const u16* wq, const u16* wuk, const u16* wg, const u16* wu, const u16* wd,
               u16* out, char* ws, u32 pf) {
    const int tid = threadIdx.x, l = tid & 63, w = __builtin_amdgcn_readfirstlane(tid >> 6);
    const int k = blockIdx.x, xc = k & 7, ii = k >> 3, h = 4 * xc + (ii >> 2), cq = ii & 3;
    const int n = l & 15, g = l >> 4;
    STAMP_DECL;
    STAMP(0);
    const rsrc_t rws = mkrs(ws, WS_END);
    const u32 E = __hip_atomic_load((const u32*)ws, __ATOMIC_RELAXED, __HIP_MEMORY_SCOPE_AGENT);
    u32x2 xv[3];
    {
        const rsrc_t rx = mkrs(x, 4 * 576 * 2);
        const u32 vo = (w < 4) ? (u32)(w * 1152 + l * 8) : OOB;
        xv[0] = __builtin_amdgcn_raw_buffer_load_b64(rx, vo, 0, 0);
        xv[1] = __builtin_amdgcn_raw_buffer_load_b64(rx, vo, 512, 0);
        xv[2] = __builtin_amdgcn_raw_buffer_load_b64(rx, l < 16 ? vo : OOB, 1024, 0);
    }
    const rsrc_t rq = mkrs(wq, 576 * 4096 * 2);
    const int rot = (9 * cq) >> 1;
    int b0 = w + rot; if (b0 >= 18) b0 -= 18;
    int b1 = w + 8 + rot; if (b1 >= 18) b1 -= 18;
    int b2 = w + 16 + rot; if (b2 >= 18) b2 -= 18;
    const u32 vq = (u32)(8 * g * 8192 + h * 256 + n * 16);
    u32x4 r0[8], r1[8], r2[8];
    u32x4 uk[4];
    ld8<AUX_WQ>(r0, rq, vq, b0 * 262144, 8192);
    {
        const rsrc_t ru = mkrs(wuk, 512 * 4096 * 2);
        const u32 vo = (u32)(((128 * cq + 16 * w + (n & 7)) * 32 + h) * 256 + ((n >> 3) * 4 + g) * 16);
#pragma unroll
        for (int s = 0; s < 4; s++) uk[s] = ldb<AUX_WUK>(ru, vo + (s & 1) * 128, (s >> 1) * 8 * 8192);
    }
    pin();
    if (w < 4) {
        float v[12];
#pragma unroll
        for (int i = 0; i < 3; i++) { v[4 * i] = lo2f(xv[i][0]); v[4 * i + 1] = hi2f(xv[i][0]); v[4 * i + 2] = lo2f(xv[i][1]); v[4 * i + 3] = hi2f(xv[i][1]); }
        float s = 0;
#pragma unroll
        for (int i = 0; i < 12; i++) s += v[i];
        const float mf = wave_sum(s) * (1.0f / 576);
        const float m = rbf(mf);
        float q = 0;
#pragma unroll
        for (int i = 0; i < 8; i++) { float d = v[i] - mf; q += d * d; }
        if (l < 16) {
#pragma unroll
            for (int i = 8; i < 12; i++) { float d = v[i] - mf; q += d * d; }
        }
        const float var = rbf(wave_sum(q) * (1.0f / 576));
        const float den = rbf(sqrtf(rbf(var + 1e-5f)));
#pragma unroll
        for (int i = 0; i < 3; i++) {
            u32x2 o;
            o[0] = pk2(rbf(v[4 * i] - m) / den, rbf(v[4 * i + 1] - m) / den);
            o[1] = pk2(rbf(v[4 * i + 2] - m) / den, rbf(v[4 * i + 3] - m) / den);
            if (i < 2 || l < 16) *(u32x2*)(smem + H_XN + w * XN_STRIDE + i * 512 + l * 8) = o;
        }
    }
    pin();
    STAMP(8);
    ld8<AUX_WQ>(r1, rq, vq, b1 * 262144, 8192);
    ld8<AUX_WQ>(r2, rq, w < 2 ? vq : OOB, b2 * 262144, 8192);
    STAMP(9);
    u32 clf = __builtin_amdgcn_raw_buffer_load_b32(rws, (u32)(k * 128), WS_CL, AUX_SC1);
    pin();
    bar();
    STAMP(1);
    {
        f32x4 acc[8];
#pragma unroll
        for (int j = 0; j < 8; j++) acc[j] = f32x4{0, 0, 0, 0};
        const char* xa = smem + H_XN + (n >> 2) * XN_STRIDE + g * 16;
        gemv8(r0, lds128(xa + b0 * 64), acc);
        gemv8(r1, lds128(xa + b1 * 64), acc);
        if (w < 2) gemv8(r2, lds128(xa + b2 * 64), acc);
        float* red = (float*)(smem + H_RED) + (w * 4 + g) * 128 + 4 * n;
        *(f32x4*)red = f32x4{acc[0][0], acc[1][0], acc[2][0], acc[3][0]};
        *(f32x4*)(red + 64) = f32x4{acc[4][0], acc[5][0], acc[6][0], acc[7][0]};
    }
    asm volatile("" :: "v"(pf));
    STAMP(2);
    bar();
    {
        const float* red = (const float*)(smem + H_RED) + tid;
        float s = 0;
#pragma unroll
        for (int i = 0; i < 8; i++) s += red[i * 512];
        const int b = tid >> 7, idx = tid & 127, d = 8 * ((idx >> 2) & 15) + 4 * (idx >> 6) + (idx & 3);
        *(u16*)(smem + H_QB + b * 320 + d * 2) = (u16)pk2(s, 0.f);
    }
    bar();
    const u32 T = E + 1;
    {
        const char* qa = smem + H_QB + (n & 3) * 320 + ((n >> 2) & 1) * 64 + g * 16;
        const u32x4 q0 = lds128(qa), q1 = lds128(qa + 128);
        f32x4 a0 = mf16(uk[0], q0, f32x4{0, 0, 0, 0}), a1 = mf16(uk[2], q0, f32x4{0, 0, 0, 0});
        a0 = mf16(uk[1], q1, a0); a1 = mf16(uk[3], q1, a1);
        if (n < 8 && (n >> 2) == (g >> 1)) {
            float* st = (float*)(smem + H_ST) + (g >> 1) * 512 + (n & 3) * 128 + 16 * w + 4 * (g & 1);
            *(f32x4*)st = a0; *(f32x4*)(st + 8) = a1;
        }
        if (w < 2) {
            for (int it = 0; it < SPIN_MAX && clf != T; it++) { __builtin_amdgcn_s_sleep(1); clf = __builtin_amdgcn_raw_buffer_load_b32(rws, (u32)(k * 128), WS_CL, AUX_SC1); }
        }
        bar();
        if (w < 2) {
            const float* st = (const float*)(smem + H_ST) + tid * 4;
            const f32x4 v = *(const f32x4*)st + *(const f32x4*)(st + 512);
            const float sc = 0.08838834764831845f * 1.4426950408889634f;
            const u32 tg = T & 0xffffu;
            u32x4 o;
#pragma unroll
            for (int r = 0; r < 4; r++) o[r] = (pk2(0.f, v[r] * sc) & 0xffff0000u) | tg;
            stb(o, rws, (u32)((((tid >> 5) * 4 + cq) * 32 + h) * 512 + (tid & 31) * 16), WS_QA);
        }
    }
    STAMP(3);
    const u32 tg12 = T & 0xfffu;
    if (k >= 56) {
        const int u = k - 56;
        const bool gu3 = (w & 3) == 0, dn = !gu3 && w != 7;
        const int t = w < 4 ? w - 1 : w - 2;
        const rsrc_t rg = mkrs(w < 4 ? wg : wu, 576 * 4608 * 2);
        const rsrc_t rd = mkrs(gu3 ? (w < 4 ? wg : wu) : wd, 576 * 4608 * 2);
        u32x4 f0[8], f1[8], f2[8], f3[8];
        const u32 vg = (u32)(((n >> 3) * 32 + 8 * g) * 9216 + (n & 7) * 16 + u * 128);
        const u32 vd = (!dn || (t == 4 && n >= 8)) ? OOB : (u32)(8 * g * 1152 + n * 16 + t * 256);
        ld8<AUX_FFN>(f0, rg, vg, (w & 3) * 128 * 9216, 9216);
        ld8<AUX_FFN>(f1, rg, vg, ((w & 3) * 128 + 64) * 9216, 9216);
        ld8<AUX_FFN>(f2, rd, gu3 ? vg : vd, gu3 ? 512 * 9216 : u * 64 * 1152, gu3 ? 9216 : 1152);
        ld8<AUX_FFN>(f3, rd, vd, (u * 64 + 32) * 1152, 1152);
        pin();
        STAMP(10);
        const char* xa = smem + H_XN + (n & 3) * XN_STRIDE + (((n >> 2) & 1) * 32 + 8 * g) * 2;
        float* red = (float*)(smem + H_RED);
        {
            f32x4 acc[8];
#pragma unroll
            for (int j = 0; j < 8; j++) acc[j] = f32x4{0, 0, 0, 0};
            gemv8(f0, lds128(xa + (w & 3) * 256), acc);
            gemv8(f1, lds128(xa + (w & 3) * 256 + 128), acc);
            if (gu3) gemv8(f2, lds128(xa + 1024), acc);
            if ((g & 1) == (n >> 3)) {
                float* p = red + ((w * 2 + (n >> 3)) * 4 + (g >> 1) * 2) * 64 + (n & 7) * 8;
                const bool up = g >= 2;
#pragma unroll
                for (int b = 0; b < 2; b++) {
                    *(f32x4*)(p + b * 64) = up ? f32x4{acc[0][2 + b], acc[1][2 + b], acc[2][2 + b], acc[3][2 + b]} : f32x4{acc[0][b], acc[1][b], acc[2][b], acc[3][b]};
                    *(f32x4*)(p + b * 64 + 4) = up ? f32x4{acc[4][2 + b], acc[5][2 + b], acc[6][2 + b], acc[7][2 + b]} : f32x4{acc[4][b], acc[5][b], acc[6][b], acc[7][b]};
                }
            }
        }
        bar();
        STAMP(11);
        if (tid < 256) {
            float gs = 0, us = 0;
#pragma unroll
            for (int i = 0; i < 8; i++) { gs += red[i * 256 + tid]; us += red[(8 + i) * 256 + tid]; }
            const float gg = rbf(gs), uu = rbf(us);
            const float inner = 0.7978845608028654f * (gg + 0.044715f * gg * gg * gg);
            const float ge = rbf(0.5f * gg * (1.0f + tanhf(inner)));
            *(u16*)(smem + H_HB + (tid >> 6) * 144 + (tid & 63) * 2) = (u16)pk2(ge * uu, 0.f);
        }
        bar();
        if (dn) {
            f32x4 acc[8];
#pragma unroll
            for (int j = 0; j < 8; j++) acc[j] = f32x4{0, 0, 0, 0};
            const char* ha = smem + H_HB + (n >> 2) * 144 + g * 16;
            gemv8(f2, lds128(ha), acc);
            gemv8(f3, lds128(ha + 64), acc);
            if (t < 4 || n < 8) {
                const bool up = (n & 4) != 0;
                u32x4 va, vb;
#pragma unroll
                for (int e = 0; e < 4; e++) {
                    const u32 lo = tag12(acc[e][0], tg12), hi = tag12(acc[4 + e][0], tg12);
                    const u32 fr = __builtin_amdgcn_update_dpp(0, hi, 0x114, 0xf, 0xf, true);
                    const u32 fl = __builtin_amdgcn_update_dpp(0, hi, 0x104, 0xf, 0xf, true);
                    va[e] = up ? fr : lo; vb[e] = up ? lo : fl;
                }
                const u32 vo = (u32)(((g * 18 + 4 * t + 2 * (n >> 3)) * 72 + u) * 128 + (n & 3) * 32);
                stb(va, rws, vo + (up ? 16 : 0), WS_YP);
                stb(vb, rws, vo + 72 * 128 + (up ? 0 : 16), WS_YP);
            }
        }
    }
    STAMP(4);
    if (k >= 56) {
        const int r = k - 56, b = r / 18, m0 = (r - b * 18) * 32;
        float xres = 0.f;
        if (tid < 32) xres = lo2f(x[b * 576 + m0 + tid]);
        f32x4 ys = {0, 0, 0, 0}, ss = {0, 0, 0, 0};
        {
            const u32 vo = (u32)(r * 72 * 128 + tid * 16);
            for (int it = 0; it < SPIN_MAX; it++) {
                u32x4 a = ldb<AUX_SC1>(rws, vo, WS_YP);
                u32x4 d = ldb<AUX_SC1>(rws, tid < 64 ? vo + 8192 : OOB, WS_YP);
                u32 bad = 0;
#pragma unroll
                for (int e = 0; e < 4; e++) bad |= (a[e] ^ tg12) | (tid < 64 ? (d[e] ^ tg12) : 0u);
                if (__all((bad & 0xfffu) == 0) || it == SPIN_MAX - 1) {
#pragma unroll
                    for (int e = 0; e < 4; e++) ys[e] = __uint_as_float(a[e] & 0xfffff000u) + __uint_as_float(d[e] & 0xfffff000u);
                    break;
                }
                __builtin_amdgcn_s_sleep(4);
            }
        }
        STAMP(5);
        {
            const u32 vo = (u32)(r * 128 * 128 + tid * 16);
            for (int it = 0; it < SPIN_MAX; it++) {
                u32x4 a = ldb<AUX_SC1>(rws, vo, WS_SP), c = ldb<AUX_SC1>(rws, vo + 8192, WS_SP);
                u32 bad = 0;
#pragma unroll
                for (int e = 0; e < 4; e++) bad |= (a[e] ^ tg12) | (c[e] ^ tg12);
                if (__all((bad & 0xfffu) == 0) || it == SPIN_MAX - 1) {
#pragma unroll
                    for (int e = 0; e < 4; e++) ss[e] = __uint_as_float(a[e] & 0xfffff000u) + __uint_as_float(c[e] & 0xfffff000u);
                    break;
                }
                __builtin_amdgcn_s_sleep(1);
            }
        }
        STAMP(6);
#pragma unroll
        for (int e = 0; e < 4; e++) {
            ys[e] += dpp<0x128>(ys[e]); ss[e] += dpp<0x128>(ss[e]);
            ys[e] = swap32sum(swap16sum(ys[e])); ss[e] = swap32sum(swap16sum(ss[e]));
        }
        float* fin = (float*)(smem + H_FIN);
        if (l < 8) { *(f32x4*)(fin + w * 32 + l * 4) = ys; *(f32x4*)(fin + 256 + w * 32 + l * 4) = ss; }
        bar();
        if (tid < 32) {
            float y = 0, s = 0;
#pragma unroll
            for (int i = 0; i < 8; i++) { y += fin[i * 32 + tid]; s += fin[256 + i * 32 + tid]; }
            y = rbf(y); s = rbf(s);
            const float gt = rbf(y / (1.0f + __expf(-y)));
            out[b * 576 + m0 + tid] = (u16)pk2(xres + rbf(s * gt), 0.f);
        }
        if (k == 56 && tid == 0) __hip_atomic_store((u32*)ws, T, __ATOMIC_RELAXED, __HIP_MEMORY_SCOPE_AGENT);
        STAMP(7);
    }
    STAMP_FLUSH;
}

#define A_Q 131072
DI void dma_row(const u16* gbase, u32 voff, u32 ldsaddr) {
    u32 keep;
    asm volatile("s_mov_b32 %0, m0\n\ts_mov_b32 m0, %3\n\ts_nop 0\n\tglobal_load_lds_dwordx4 %1, %2" KV_NT "\n\ts_mov_b32 m0, %0"
                 : "=&s"(keep) : "v"(voff), "s"(gbase), "s"(ldsaddr) : "memory");
}
template <int OFF> DI void ld_sc1_asm(u32x4& r, u32 voff, const char* base) {
    asm volatile("global_load_dwordx4 %0, %1, %2 offset:%3 sc1" : "=&v"(r) : "v"(voff), "s"(base), "n"(OFF) : "memory");
}

DI void a_weights(u32x4 (&w0)[8], u32x4 (&w1)[8], u32x4 (&w2)[8], const u16* wuv, const u16* wo, int w, int h, int al, int n, int g) {
    const int kb = w & 3;
    const rsrc_t ro = mkrs(wo, 4096 * 576 * 2);
    const u32 vo = (u32)(8 * g * 1152 + n * 16);
    const u32 so = (u32)((h * 128 + 32 * kb) * 1152);
    if (w < 4) {
        const rsrc_t rv = mkrs(wuv, 512 * 4096 * 2);
        ld8<AUX_WUV>(w0, rv, (u32)(8 * g * 8192 + h * 256 + n * 16), (128 * al + 32 * kb) * 8192, 8192);
    } else {
        ld8<AUX_WO>(w0, ro, vo, so, 1152);
    }
    ld8<AUX_WO>(w1, ro, vo, so + (1 + (w >> 2)) * 256, 1152);
    ld8<AUX_WO>(w2, ro, (w >= 4 && n >= 8) ? OOB : vo, so + (3 + (w >> 2)) * 256, 1152);
}

DI void a_role(char* smem, const u16* ckv, const u16* wuv, const u16* wo, char* ws, u32 pf) {
    const int tid = threadIdx.x, l = tid & 63, w = __builtin_amdgcn_readfirstlane(tid >> 6);
    const int a = blockIdx.x - 128, xc = a & 7, ii = a >> 3;
    const int b = xc >> 1, chunk = (xc & 1) * 16 + ii;
    const int h = 4 * xc + (ii >> 2), al = ii & 3;
    const int n = l & 15, g = l >> 4, hh = l >> 5;
    STAMP_DECL;
    STAMP(0);
    const rsrc_t rws = mkrs(ws, WS_END);
    u32 E;
    asm volatile("global_load_dword %0, %1, %2 sc1" : "=&v"(E) : "v"(0u), "s"(ws) : "memory");
    {
        const u32x4 ones = {~0u, ~0u, ~0u, ~0u};
#pragma unroll
        for (int i = 0; i < 4; i++) stb(ones, rws, (u32)(tid * 16 + i * 8192), WS_PART + (h * 4 + al) * 32768);
        stb(ones, rws, tid < 16 ? (u32)(tid * 16 + a * 256 + WS_ST) : OOB, 0);
    }
    const u32 lds0 = (u32)(size_t)(LAS char*)smem;
    const u16* kb0 = ckv + ((size_t)b * 4096 + chunk * 128 + 16 * w) * 512;
    const u32 ldsw = lds0 + w * 16384;
#pragma unroll
    for (int i = 0; i < CLR_EARLY; i++) dma_row(kb0, (u32)(((l ^ (((i & 3) << 2) | (i >> 2))) << 4) + i * 1024), ldsw + i * 1024);
    asm volatile("s_waitcnt vmcnt(%1)" : "+v"(E) : "n"(CLR_EARLY), "v"(pf) : "memory");
    const u32 T = E + 1;
    bar();
    if (tid == 0) __builtin_amdgcn_raw_buffer_store_b32(T, rws, (u32)(a * 128 + WS_CL), 0, AUX_SC1);
#pragma unroll
    for (int i = CLR_EARLY; i < 16; i++) dma_row(kb0, (u32)(((l ^ (((i & 3) << 2) | (i >> 2))) << 4) + i * 1024), ldsw + i * 1024);
    u32x4 w0[8], w1[8], w2[8];
    if (w < 4) a_weights(w0, w1, w2, wuv, wo, w, h, al, n, g);
    asm volatile("s_waitcnt vmcnt(0)" ::: "memory");
    {
        const char* qsrc = ws + WS_QA + b * 65536 + w * 8192;
        const u32 vq = l * 16 + 4096;
        const u32 tg = T & 0xffffu;
        u32x4 q[8];
        STAMP(1);
        for (int it = 0; it < SPIN_MAX; it++) {
            ld_sc1_asm<-4096>(q[0], vq, qsrc); ld_sc1_asm<-3072>(q[1], vq, qsrc);
            ld_sc1_asm<-2048>(q[2], vq, qsrc); ld_sc1_asm<-1024>(q[3], vq, qsrc);
            ld_sc1_asm<0>(q[4], vq, qsrc); ld_sc1_asm<1024>(q[5], vq, qsrc);
            ld_sc1_asm<2048>(q[6], vq, qsrc); ld_sc1_asm<3072>(q[7], vq, qsrc);
            asm volatile("s_waitcnt vmcnt(0)" : "+v"(q[0]), "+v"(q[1]), "+v"(q[2]), "+v"(q[3]), "+v"(q[4]), "+v"(q[5]), "+v"(q[6]), "+v"(q[7]) :: "memory");
            u32 bad = 0;
#pragma unroll
            for (int i = 0; i < 8; i++) bad |= (q[i][0] ^ tg) | (q[i][1] ^ tg) | (q[i][2] ^ tg) | (q[i][3] ^ tg);
            if (__all((bad & 0xffffu) == 0)) break;
            __builtin_amdgcn_s_sleep(POLL_SLEEP);
        }
        STAMP(2);
        const int z = (l >> 1) & 7;
        char* qd = smem + A_Q + (8 * (w >> 1) + ((l & 31) >> 2)) * 1024 + ((l >> 1) & 1) * 512 + (l & 1) * 8;
#pragma unroll
        for (int i = 0; i < 8; i++) {
            const int hq = 16 * (w & 1) + 2 * i + hh;
            u32x2 o = {__builtin_amdgcn_perm(q[i][1], q[i][0], 0x07060302u), __builtin_amdgcn_perm(q[i][3], q[i][2], 0x07060302u)};
            *(u32x2*)(qd + ((hq ^ z) << 4)) = o;
        }
    }
    bar();
    STAMP(3);
    if (w >= 4) { a_weights(w0, w1, w2, wuv, wo, w, h, al, n, g); pin(); }
    f32x16 sacc;
    float mx = 0.f;
    if (w < 4) {
#pragma unroll
        for (int r = 0; r < 16; r++) sacc[r] = 0.f;
        const int row = 32 * w + (l & 31);
        const int f = ((row & 3) << 2) | ((row >> 2) & 3);
        const char* kp = smem + row * 1024;
        const int fx = (f ^ hh) << 4;
        const char* qp = smem + A_Q + hh * 512;
#pragma unroll
        for (int s = 0; s < 32; s++) {
            const u32x4 ka = lds128(kp + ((32 * (s & 7)) ^ fx) + 256 * (s >> 3));
            const u32x4 qb = lds128(qp + s * 1024 + (((l & 31) ^ ((s & 3) * 2 + hh)) << 4));
            sacc = mf32(ka, qb, sacc);
        }
        mx = sacc[0];
#pragma unroll
        for (int r = 1; r < 16; r++) mx = fmaxf(mx, sacc[r]);
        mx = swap32max(mx);
    }
    bar();
    float* aux = (float*)(smem + A_Q);
    if (w < 4 && l < 32) aux[w * 32 + l] = mx;
    bar();
    if (w < 4) {
        const int hq = l & 31;
        const float M = fmaxf(fmaxf(aux[hq], aux[32 + hq]), fmaxf(aux[64 + hq], aux[96 + hq]));
        float p[16], ls = 0.f;
#pragma unroll
        for (int r = 0; r < 16; r++) { p[r] = __builtin_amdgcn_exp2f(sacc[r] - M); ls += p[r]; }
        ls = swap32sum(ls);
        if (l < 32) aux[128 + w * 32 + l] = ls;
        char* pd = smem + A_Q + 1024 + (2 * w) * 1024 + l * 16;
        *(u32x4*)pd = u32x4{pk2(p[0], p[1]), pk2(p[2], p[3]), pk2(p[4], p[5]), pk2(p[6], p[7])};
        *(u32x4*)(pd + 1024) = u32x4{pk2(p[8], p[9]), pk2(p[10], p[11]), pk2(p[12], p[13]), pk2(p[14], p[15])};
    }
    bar();
    STAMP(4);
    if (w == 4 && l < 16) {
        u32x4 o;
#pragma unroll
        for (int e = 0; e < 2; e++) {
            const int hq = 2 * l + e;
            const float M = fmaxf(fmaxf(aux[hq], aux[32 + hq]), fmaxf(aux[64 + hq], aux[96 + hq]));
            const float L = (aux[128 + hq] + aux[160 + hq]) + (aux[192 + hq] + aux[224 + hq]);
            o[2 * e] = min(__float_as_uint(M), 0xfffffffeu); o[2 * e + 1] = min(__float_as_uint(L), 0xfffffffeu);
        }
        stb(o, rws, (u32)((b * 32 + chunk) * 256 + l * 16), WS_ST);
    }
    {
        f32x16 o0, o1;
#pragma unroll
        for (int r = 0; r < 16; r++) { o0[r] = 0.f; o1[r] = 0.f; }
        const int q4 = (l & 15) >> 2, p4 = l & 3, blk = (l >> 4) & 1;
        const char* vb = smem + (4 * hh + q4) * 1024 + (w >> 1) * 256 + (p4 >> 1) * 8;
        const int cl = 8 * (w & 1) + 2 * blk + (p4 & 1);
        const char* pp = smem + A_Q + 1024 + l * 16;
#pragma unroll
        for (int ks = 0; ks < 8; ks++) {
            const u32x4 pb = lds128(pp + ks * 1024);
            u32x4 va[2];
#pragma unroll
            for (int cb = 0; cb < 2; cb++) {
#pragma unroll
                for (int t = 0; t < 2; t++) {
                    const int f = (q4 << 2) | (hh + 2 * t);
                    const char* ad = vb + ks * 16384 + t * 8192 + (((cl + 4 * cb) ^ f) << 4);
                    s16x4 v = __builtin_amdgcn_ds_read_tr16_b64_v4i16((LAS s16x4*)ad);
                    u32x2 vv = __builtin_bit_cast(u32x2, v);
                    va[cb][2 * t] = vv[0]; va[cb][2 * t + 1] = vv[1];
                }
            }
            o0 = mf32(va[0], pb, o0);
            o1 = mf32(va[1], pb, o1);
        }
        STAMP(5);
        u32x4 s0[2], s1[2];
#pragma unroll
        for (int i = 0; i < 2; i++) {
#pragma unroll
            for (int e = 0; e < 4; e++) {
                s0[i][e] = min(pk2(o0[8 * i + 2 * e], o0[8 * i + 2 * e + 1]), 0xfffffffeu);
                s1[i][e] = min(pk2(o1[8 * i + 2 * e], o1[8 * i + 2 * e + 1]), 0xfffffffeu);
            }
        }
        char* stg = smem + A_Q + 9216 + w * 2304;
        char* sw = stg + (l & 15) * 144 + hh * 16;
        const char* sr = stg + (l >> 3) * 144 + (l & 7) * 16;
        const u32 vo = (u32)((((l >> 3) * 4 + (w >> 1)) * 128 + b * 32 + chunk) * 256 + (w & 1) * 128 + (l & 7) * 16);
#pragma unroll
        for (int rd = 0; rd < 2; rd++) {
            if (((l >> 4) & 1) == rd) {
                *(u32x4*)(sw) = s0[0]; *(u32x4*)(sw + 32) = s0[1]; *(u32x4*)(sw + 64) = s1[0]; *(u32x4*)(sw + 96) = s1[1];
            }
            asm volatile("" ::: "memory");
            const u32x4 v0 = lds128(sr), v1 = lds128(sr + 8 * 144);
            stb(v0, rws, vo + (16 * rd) * 131072, WS_PART);
            stb(v1, rws, vo + (16 * rd + 8) * 131072, WS_PART);
            asm volatile("" ::: "memory");
        }
    }
    STAMP(6);
    u32x4 t0[8], t1[8], t2[8];
    tr8(w0, t0); tr8(w1, t1); tr8(w2, t2);
#pragma unroll
    for (int j = 0; j < 8; j++) asm volatile("" : "+v"(t0[j]), "+v"(t1[j]), "+v"(t2[j]));
    STAMP(7);
#define D_WT (A_Q + 28672)
#define D_OB 4096
#define D_CX 8192
#define D_SR 16384
    u32x4 pt[8];
    if (w < 4) {
        const u32 vo = (u32)((w * 32 + 8 * g) * 256 + n * 16);
        const u32 so = WS_PART + (h * 4 + al) * 32768;
        u32x4 pr[8];
        for (int it = 0; it < SPIN_MAX; it++) {
#pragma unroll
            for (int i = 0; i < 8; i++) pr[i] = ldb<AUX_SC1>(rws, vo + i * 256, so);
            u32 mxv = 0;
#pragma unroll
            for (int i = 0; i < 8; i++) mxv = max(max(mxv, max(pr[i][0], pr[i][1])), max(pr[i][2], pr[i][3]));
            if (__all(mxv != 0xffffffffu)) break;
            __builtin_amdgcn_s_sleep(1);
        }
        tr8(pr, pt);
    }
    if (w == 4 || w == 5) {
        const u32 vo = (u32)(((2 * (w - 4) + hh) * 32 + (l & 31)) * 256 + h * 8);
        u32x2 sr;
        for (int it = 0; it < SPIN_MAX; it++) {
            sr = __builtin_amdgcn_raw_buffer_load_b64(rws, vo, WS_ST, AUX_SC1);
            if (__all(max(sr[0], sr[1]) != 0xffffffffu)) break;
            __builtin_amdgcn_s_sleep(1);
        }
        const float m = __uint_as_float(sr[0]);
        float M = m;
        M = fmaxf(M, dpp<0xb1>(M)); M = fmaxf(M, dpp<0x4e>(M)); M = fmaxf(M, dpp<0x141>(M)); M = fmaxf(M, dpp<0x140>(M)); M = swap16max(M);
        const float e = __builtin_amdgcn_exp2f(m - M);
        float L = e * __uint_as_float(sr[1]);
        L += dpp<0xb1>(L); L += dpp<0x4e>(L); L += dpp<0x141>(L); L += dpp<0x140>(L); L = swap16sum(L);
        const float a = e / L;
        const u32 hi = pk2(a, 0.f);
        const u32 lo = pk2(a - lo2f(hi), 0.f);
        u16* wt = (u16*)(smem + D_WT) + (2 * (w - 4) + hh) * 64 + (l & 31);
        wt[0] = (u16)hi; wt[32] = (u16)lo;
    }
    bar();
    STAMP(8);
    if (w < 4) {
        u32x4 aw = lds128(smem + D_WT + w * 128 + (n & 1) * 64 + g * 16);
        if (n >= 2) aw = u32x4{0, 0, 0, 0};
        float o[8];
#pragma unroll
        for (int j = 0; j < 8; j++) { f32x4 d = mf16(aw, pt[j], f32x4{0, 0, 0, 0}); o[j] = d[0] + d[1]; }
        if (g == 0) *(u32x4*)(smem + D_OB + w * 320 + n * 16) = u32x4{pk2(o[0], o[1]), pk2(o[2], o[3]), pk2(o[4], o[5]), pk2(o[6], o[7])};
    }
    bar();
    if (w < 4) {
        const u32x4 ao = lds128(smem + D_OB + (n >> 2) * 320 + (32 * w + 8 * g) * 2);
        f32x4 d[8];
#pragma unroll
        for (int j = 0; j < 8; j++) d[j] = mf16(ao, t0[j], f32x4{0, 0, 0, 0});
        float* cx = (float*)(smem + D_CX) + (w * 4 + g) * 128 + 8 * n;
        *(f32x4*)cx = f32x4{d[0][0], d[1][0], d[2][0], d[3][0]};
        *(f32x4*)(cx + 4) = f32x4{d[4][0], d[5][0], d[6][0], d[7][0]};
    }
    bar();
    {
        const int kb = w & 3;
        const float* cx = (const float*)(smem + D_CX) + (n >> 2) * 128 + 32 * kb + 8 * g;
        f32x4 lo = *(const f32x4*)cx, hi = *(const f32x4*)(cx + 4);
#pragma unroll
        for (int i = 1; i < 4; i++) { lo += *(const f32x4*)(cx + i * 512); hi += *(const f32x4*)(cx + i * 512 + 4); }
        const u32x4 ac = {pk2(lo[0], lo[1]), pk2(lo[2], lo[3]), pk2(hi[0], hi[1]), pk2(hi[2], hi[3])};
        float* sr = (float*)(smem + D_SR) + (kb * 4 + g) * 640 + 8 * n;
        f32x4 d[8];
        if (w >= 4) {
#pragma unroll
            for (int j = 0; j < 8; j++) d[j] = mf16(ac, t0[j], f32x4{0, 0, 0, 0});
            *(f32x4*)sr = f32x4{d[0][0], d[1][0], d[2][0], d[3][0]};
            *(f32x4*)(sr + 4) = f32x4{d[4][0], d[5][0], d[6][0], d[7][0]};
        }
#pragma unroll
        for (int j = 0; j < 8; j++) d[j] = mf16(ac, t1[j], f32x4{0, 0, 0, 0});
        float* s1 = sr + (1 + (w >> 2)) * 128;
        *(f32x4*)s1 = f32x4{d[0][0], d[1][0], d[2][0], d[3][0]};
        *(f32x4*)(s1 + 4) = f32x4{d[4][0], d[5][0], d[6][0], d[7][0]};
#pragma unroll
        for (int j = 0; j < 8; j++) d[j] = mf16(ac, t2[j], f32x4{0, 0, 0, 0});
        float* s2 = sr + (3 + (w >> 2)) * 128;
        *(f32x4*)s2 = f32x4{d[0][0], d[1][0], d[2][0], d[3][0]};
        *(f32x4*)(s2 + 4) = f32x4{d[4][0], d[5][0], d[6][0], d[7][0]};
    }
    bar();
    {
        const u32 tg12 = T & 0xfffu;
#pragma unroll
        for (int i = 0; i < 2; i++) {
            const int ch = tid + 512 * i;
            if (ch < 576) {
                const int bb = ch / 144, m = (ch - bb * 144) * 4;
                const float* sr = (const float*)(smem + D_SR) + bb * 640 + m;
                f32x4 s = *(const f32x4*)sr;
#pragma unroll
                for (int q = 1; q < 4; q++) s += *(const f32x4*)(sr + q * 2560);
                stb(u32x4{tag12(s[0], tg12), tag12(s[1], tg12), tag12(s[2], tg12), tag12(s[3], tg12)}, rws,
                    (u32)(((bb * 18 + (m >> 5)) * 128 + a) * 128 + (m & 31) * 4), WS_SP);
            }
        }
    }
    STAMP(9);
    STAMP_FLUSH;
}

#define IPF 0
template <int V>
__global__ __launch_bounds__(512, 2) void fused_decode(const u16* x, const u16* ckv, const u16* wq, const u16* wuk, const u16* wuv,
                                                       const u16* wo, const u16* wg, const u16* wu, const u16* wd, u16* out, char* ws) {
    extern __shared__ __attribute__((aligned(16))) char smem[];
    u32 pf = 0;
    if (blockIdx.x < 128) h_role(smem, x, wq, wuk, wg, wu, wd, out, ws, pf);
    else a_role(smem, ckv, wuv, wo, ws, pf);
}

extern "C" void kernel_launch(void* const* d_in, const int* in_sizes, int n_in, void* d_out, int out_size, void* d_ws, size_t ws_size,
                              hipStream_t stream) {
    (void)in_sizes; (void)n_in; (void)out_size; (void)ws_size;
    hipFuncSetAttribute((const void*)fused_decode<0>, hipFuncAttributeMaxDynamicSharedMemorySize, 163840);
    fused_decode<0><<<256, 512, 163840, stream>>>((const u16*)d_in[0], (const u16*)d_in[1], (const u16*)d_in[2], (const u16*)d_in[3],
                                                  (const u16*)d_in[4], (const u16*)d_in[5], (const u16*)d_in[7], (const u16*)d_in[8],
                                                  (const u16*)d_in[9], (u16*)d_out, (char*)d_ws);
}
```

```cpp
#include <hip/hip_runtime.h>
typedef unsigned short u16;
typedef unsigned int u32;
typedef unsigned long long u64;
typedef u32 u32x4 __attribute__((ext_vector_type(4)));
typedef u32 u32x2 __attribute__((ext_vector_type(2)));
typedef float f32x4 __attribute__((ext_vector_type(4)));
typedef float f32x2 __attribute__((ext_vector_type(2)));
typedef float f32x16 __attribute__((ext_vector_type(16)));
typedef __bf16 bf16x8 __attribute__((ext_vector_type(8)));
typedef __bf16 bf16x2 __attribute__((ext_vector_type(2)));
typedef short s16x4 __attribute__((ext_vector_type(4)));
typedef __amdgpu_buffer_rsrc_t rsrc_t;
#define DI __device__ __forceinline__
#define LAS __attribute__((address_space(3)))

#define WS_QA   4096u
#define WS_PART (WS_QA + 262144u)
#define WS_ST   (WS_PART + 4194304u)
#define WS_SP   (WS_ST + 32768u)
#define WS_YP   (WS_SP + 1179648u)
#define WS_CL   (WS_YP + 1327104u)
#define WS_END  (WS_CL + 16384u)
#define WS_DBG  (8u << 20)

#define SPIN_MAX 20000
#define AUX_WQ 0
#define AUX_WUK 0
#define AUX_FFN 2
#define AUX_WUV 2
#define AUX_WO 0
#define KV_NT " nt"
#define DMA_SPLIT 12
#define DMA_WAIT 4
#define ZPRIME 1
#define FFN_DELAY 0
#define A_DELAY 0
#define CLR_EARLY 8
#define POLL_SLEEP 1
#define HORD 1
#define FLOOR_AT 5
#define FLOOR_T 800
#define FLOOR(id) do { if (FLOOR_AT == id) { while ((u32)__builtin_amdgcn_s_memrealtime() - t_fl < FLOOR_T) __builtin_amdgcn_s_sleep(2); } } while (0)
#define AUX_SC1 16
#define OOB 0x80000000u

#define STAMP_DECL
#define STAMP(i) do { } while (0)
#define STAMP_FLUSH

DI float lo2f(u32 v) { return __uint_as_float(v << 16); }
DI float hi2f(u32 v) { return __uint_as_float(v & 0xffff0000u); }
DI u32 pk2(float lo, float hi) { f32x2 v = {lo, hi}; return __builtin_bit_cast(u32, __builtin_convertvector(v, bf16x2)); }
DI float rbf(float f) { return lo2f(pk2(f, 0.f)); }
DI rsrc_t mkrs(const void* p, u32 bytes) { return __builtin_amdgcn_make_buffer_rsrc((void*)p, 0, (int)bytes, 0x00020000); }
template <int AUX> DI u32x4 ldb(rsrc_t rs, u32 voff, u32 soff) { return __builtin_amdgcn_raw_buffer_load_b128(rs, voff, soff, AUX); }
DI void stb(u32x4 v, rsrc_t rs, u32 voff, u32 soff) { __builtin_amdgcn_raw_buffer_store_b128(v, rs, voff + soff, 0, AUX_SC1); }
DI void bar() { asm volatile("s_waitcnt lgkmcnt(0)\n\ts_barrier" ::: "memory"); }
DI void pin() { asm volatile("" ::: "memory"); }
DI f32x4 mf16(u32x4 a, u32x4 b, f32x4 c) { return __builtin_amdgcn_mfma_f32_16x16x32_bf16(__builtin_bit_cast(bf16x8, a), __builtin_bit_cast(bf16x8, b), c, 0, 0, 0); }
DI f32x16 mf32(u32x4 a, u32x4 b, f32x16 c) { return __builtin_amdgcn_mfma_f32_32x32x16_bf16(__builtin_bit_cast(bf16x8, a), __builtin_bit_cast(bf16x8, b), c, 0, 0, 0); }
DI float swap32max(float v) { auto r = __builtin_amdgcn_permlane32_swap(__float_as_uint(v), __float_as_uint(v), false, false); return fmaxf(__uint_as_float(r[0]), __uint_as_float(r[1])); }
DI float swap32sum(float v) { auto r = __builtin_amdgcn_permlane32_swap(__float_as_uint(v), __float_as_uint(v), false, false); return __uint_as_float(r[0]) + __uint_as_float(r[1]); }
DI float swap16max(float v) { auto r = __builtin_amdgcn_permlane16_swap(__float_as_uint(v), __float_as_uint(v), false, false); return fmaxf(__uint_as_float(r[0]), __uint_as_float(r[1])); }
DI float swap16sum(float v) { auto r = __builtin_amdgcn_permlane16_swap(__float_as_uint(v), __float_as_uint(v), false, false); return __uint_as_float(r[0]) + __uint_as_float(r[1]); }
template <int CTRL> DI float dpp(float v) { return __uint_as_float(__builtin_amdgcn_update_dpp(0, __float_as_uint(v), CTRL, 0xf, 0xf, true)); }
DI float wave_sum(float v) {
    v += dpp<0xb1>(v);
    v += dpp<0x4e>(v);
    v += dpp<0x141>(v);
    v += dpp<0x140>(v);
    v = swap16sum(v);
    return swap32sum(v);
}
DI void tr8(const u32x4 (&r)[8], u32x4 (&t)[8]) {
#pragma unroll
    for (int jj = 0; jj < 4; jj++) {
#pragma unroll
        for (int p = 0; p < 4; p++) {
            t[2 * jj][p] = __builtin_amdgcn_perm(r[2 * p + 1][jj], r[2 * p][jj], 0x05040100u);
            t[2 * jj + 1][p] = __builtin_amdgcn_perm(r[2 * p + 1][jj], r[2 * p][jj], 0x07060302u);
        }
    }
}
template <int AUX> DI void ld8(u32x4 (&r)[8], rsrc_t rs, u32 voff, u32 soff, u32 stride) {
#pragma unroll
    for (int i = 0; i < 8; i++) r[i] = ldb<AUX>(rs, voff, soff + i * stride);
}
DI void gemv8(const u32x4 (&r)[8], u32x4 a, f32x4 (&acc)[8]) {
    u32x4 t[8]; tr8(r, t);
#pragma unroll
    for (int j = 0; j < 8; j++) acc[j] = mf16(a, t[j], acc[j]);
}
DI u32 tag12(float v, u32 tg) { return ((__float_as_uint(v) + 0x800u) & 0xfffff000u) | tg; }
DI u32x4 lds128(const char* p) { return *(const u32x4*)p; }

#define XN_STRIDE 1216
#define H_XN   0
#define H_RED  5120
#define H_QB   (H_RED + 20480)
#define H_HB   (H_QB + 1280)
#define H_FIN  (H_HB + 640)
#define H_ST   (H_FIN + 2048)

DI void h_role(char* smem, const u16* x, const u16* wq, const u16* wuk, const u16* wg, const u16* wu, const u16* wd,
               u16* out, char* ws, u32 pf) {
    const int tid = threadIdx.x, l = tid & 63, w = __builtin_amdgcn_readfirstlane(tid >> 6);
    const int k = blockIdx.x, xc = k & 7, ii = k >> 3, h = 4 * xc + (ii >> 2), cq = ii & 3;
    const int n = l & 15, g = l >> 4;
    STAMP_DECL;
    STAMP(0);
    const u32 t_fl = (u32)__builtin_amdgcn_s_memrealtime();
    const rsrc_t rws = mkrs(ws, WS_END);
    const u32 E = __hip_atomic_load((const u32*)ws, __ATOMIC_RELAXED, __HIP_MEMORY_SCOPE_AGENT);
    u32x2 xv[3];
    {
        const rsrc_t rx = mkrs(x, 4 * 576 * 2);
        const u32 vo = (w < 4) ? (u32)(w * 1152 + l * 8) : OOB;
        xv[0] = __builtin_amdgcn_raw_buffer_load_b64(rx, vo, 0, 0);
        xv[1] = __builtin_amdgcn_raw_buffer_load_b64(rx, vo, 512, 0);
        xv[2] = __builtin_amdgcn_raw_buffer_load_b64(rx, l < 16 ? vo : OOB, 1024, 0);
    }
    const rsrc_t rq = mkrs(wq, 576 * 4096 * 2);
    const int rot = (9 * cq) >> 1;
    int b0 = w + rot; if (b0 >= 18) b0 -= 18;
    int b1 = w + 8 + rot; if (b1 >= 18) b1 -= 18;
    int b2 = w + 16 + rot; if (b2 >= 18) b2 -= 18;
    const u32 vq = (u32)(8 * g * 8192 + h * 256 + n * 16);
    u32x4 r0[8], r1[8], r2[8];
    u32x4 uk[4];
    ld8<AUX_WQ>(r0, rq, vq, b0 * 262144, 8192);
    {
        const rsrc_t ru = mkrs(wuk, 512 * 4096 * 2);
        const u32 vo = (u32)(((128 * cq + 16 * w + (n & 7)) * 32 + h) * 256 + ((n >> 3) * 4 + g) * 16);
#pragma unroll
        for (int s = 0; s < 4; s++) uk[s] = ldb<AUX_WUK>(ru, vo + (s & 1) * 128, (s >> 1) * 8 * 8192);
    }
    pin();
    if (w < 4) {
        float v[12];
#pragma unroll
        for (int i = 0; i < 3; i++) { v[4 * i] = lo2f(xv[i][0]); v[4 * i + 1] = hi2f(xv[i][0]); v[4 * i + 2] = lo2f(xv[i][1]); v[4 * i + 3] = hi2f(xv[i][1]); }
        float s = 0;
#pragma unroll
        for (int i = 0; i < 12; i++) s += v[i];
        const float mf = wave_sum(s) * (1.0f / 576);
        const float m = rbf(mf);
        float q = 0;
#pragma unroll
        for (int i = 0; i < 8; i++) { float d = v[i] - mf; q += d * d; }
        if (l < 16) {
#pragma unroll
            for (int i = 8; i < 12; i++) { float d = v[i] - mf; q += d * d; }
        }
        const float var = rbf(wave_sum(q) * (1.0f / 576));
        const float den = rbf(sqrtf(rbf(var + 1e-5f)));
#pragma unroll
        for (int i = 0; i < 3; i++) {
            u32x2 o;
            o[0] = pk2(rbf(v[4 * i] - m) / den, rbf(v[4 * i + 1] - m) / den);
            o[1] = pk2(rbf(v[4 * i + 2] - m) / den, rbf(v[4 * i + 3] - m) / den);
            if (i < 2 || l < 16) *(u32x2*)(smem + H_XN + w * XN_STRIDE + i * 512 + l * 8) = o;
        }
    }
    pin();
    STAMP(8);
    ld8<AUX_WQ>(r1, rq, vq, b1 * 262144, 8192);
    ld8<AUX_WQ>(r2, rq, w < 2 ? vq : OOB, b2 * 262144, 8192);
    STAMP(9);
    u32 clf = __builtin_amdgcn_raw_buffer_load_b32(rws, (u32)(k * 128), WS_CL, AUX_SC1);
    pin();
    bar();
    FLOOR(5);
    STAMP(1);
    {
        f32x4 acc[8];
#pragma unroll
        for (int j = 0; j < 8; j++) acc[j] = f32x4{0, 0, 0, 0};
        const char* xa = smem + H_XN + (n >> 2) * XN_STRIDE + g * 16;
        gemv8(r0, lds128(xa + b0 * 64), acc);
        gemv8(r1, lds128(xa + b1 * 64), acc);
        if (w < 2) gemv8(r2, lds128(xa + b2 * 64), acc);
        float* red = (float*)(smem + H_RED) + (w * 4 + g) * 128 + 4 * n;
        *(f32x4*)red = f32x4{acc[0][0], acc[1][0], acc[2][0], acc[3][0]};
        *(f32x4*)(red + 64) = f32x4{acc[4][0], acc[5][0], acc[6][0], acc[7][0]};
    }
    asm volatile("" :: "v"(pf));
    STAMP(2);
    bar();
    {
        const float* red = (const float*)(smem + H_RED) + tid;
        float s = 0;
#pragma unroll
        for (int i = 0; i < 8; i++) s += red[i * 512];
        const int b = tid >> 7, idx = tid & 127, d = 8 * ((idx >> 2) & 15) + 4 * (idx >> 6) + (idx & 3);
        *(u16*)(smem + H_QB + b * 320 + d * 2) = (u16)pk2(s, 0.f);
    }
    bar();
    const u32 T = E + 1;
    {
        const char* qa = smem + H_QB + (n & 3) * 320 + ((n >> 2) & 1) * 64 + g * 16;
        const u32x4 q0 = lds128(qa), q1 = lds128(qa + 128);
        f32x4 a0 = mf16(uk[0], q0, f32x4{0, 0, 0, 0}), a1 = mf16(uk[2], q0, f32x4{0, 0, 0, 0});
        a0 = mf16(uk[1], q1, a0); a1 = mf16(uk[3], q1, a1);
        if (n < 8 && (n >> 2) == (g >> 1)) {
            float* st = (float*)(smem + H_ST) + (g >> 1) * 512 + (n & 3) * 128 + 16 * w + 4 * (g & 1);
            *(f32x4*)st = a0; *(f32x4*)(st + 8) = a1;
        }
        if (w < 2) {
            for (int it = 0; it < SPIN_MAX && clf != T; it++) { __builtin_amdgcn_s_sleep(1); clf = __builtin_amdgcn_raw_buffer_load_b32(rws, (u32)(k * 128), WS_CL, AUX_SC1); }
        }
        bar();
        FLOOR(2);
        if (w < 2) {
            const float* st = (const float*)(smem + H_ST) + tid * 4;
            const f32x4 v = *(const f32x4*)st + *(const f32x4*)(st + 512);
            const float sc = 0.08838834764831845f * 1.4426950408889634f;
            const u32 tg = T & 0xffffu;
            u32x4 o;
#pragma unroll
            for (int r = 0; r < 4; r++) o[r] = (pk2(0.f, v[r] * sc) & 0xffff0000u) | tg;
            stb(o, rws, (u32)((((tid >> 5) * 4 + cq) * 32 + h) * 512 + (tid & 31) * 16), WS_QA);
        }
    }
    STAMP(3);
    const u32 tg12 = T & 0xfffu;
    if (k >= 56) {
        const int u = k - 56;
        const bool gu3 = (w & 3) == 0, dn = !gu3 && w != 7;
        const int t = w < 4 ? w - 1 : w - 2;
        const rsrc_t rg = mkrs(w < 4 ? wg : wu, 576 * 4608 * 2);
        const rsrc_t rd = mkrs(gu3 ? (w < 4 ? wg : wu) : wd, 576 * 4608 * 2);
        u32x4 f0[8], f1[8], f2[8], f3[8];
        const u32 vg = (u32)(((n >> 3) * 32 + 8 * g) * 9216 + (n & 7) * 16 + u * 128);
        const u32 vd = (!dn || (t == 4 && n >= 8)) ? OOB : (u32)(8 * g * 1152 + n * 16 + t * 256);
        ld8<AUX_FFN>(f0, rg, vg, (w & 3) * 128 * 9216, 9216);
        ld8<AUX_FFN>(f1, rg, vg, ((w & 3) * 128 + 64) * 9216, 9216);
        ld8<AUX_FFN>(f2, rd, gu3 ? vg : vd, gu3 ? 512 * 9216 : u * 64 * 1152, gu3 ? 9216 : 1152);
        ld8<AUX_FFN>(f3, rd, vd, (u * 64 + 32) * 1152, 1152);
        pin();
        STAMP(10);
        const char* xa = smem + H_XN + (n & 3) * XN_STRIDE + (((n >> 2) & 1) * 32 + 8 * g) * 2;
        float* red = (float*)(smem + H_RED);
        {
            f32x4 acc[8];
#pragma unroll
            for (int j = 0; j < 8; j++) acc[j] = f32x4{0, 0, 0, 0};
            gemv8(f0, lds128(xa + (w & 3) * 256), acc);
            gemv8(f1, lds128(xa + (w & 3) * 256 + 128), acc);
            if (gu3) gemv8(f2, lds128(xa + 1024), acc);
            if ((g & 1) == (n >> 3)) {
                float* p = red + ((w * 2 + (n >> 3)) * 4 + (g >> 1) * 2) * 64 + (n & 7) * 8;
                const bool up = g >= 2;
#pragma unroll
                for (int b = 0; b < 2; b++) {
                    *(f32x4*)(p + b * 64) = up ? f32x4{acc[0][2 + b], acc[1][2 + b], acc[2][2 + b], acc[3][2 + b]} : f32x4{acc[0][b], acc[1][b], acc[2][b], acc[3][b]};
                    *(f32x4*)(p + b * 64 + 4) = up ? f32x4{acc[4][2 + b], acc[5][2 + b], acc[6][2 + b], acc[7][2 + b]} : f32x4{acc[4][b], acc[5][b], acc[6][b], acc[7][b]};
                }
            }
        }
        bar();
        STAMP(11);
        if (tid < 256) {
            float gs = 0, us = 0;
#pragma unroll
            for (int i = 0; i < 8; i++) { gs += red[i * 256 + tid]; us += red[(8 + i) * 256 + tid]; }
            const float gg = rbf(gs), uu = rbf(us);
            const float inner = 0.7978845608028654f * (gg + 0.044715f * gg * gg * gg);
            const float ge = rbf(0.5f * gg * (1.0f + tanhf(inner)));
            *(u16*)(smem + H_HB + (tid >> 6) * 144 + (tid & 63) * 2) = (u16)pk2(ge * uu, 0.f);
        }
        bar();
        if (dn) {
            f32x4 acc[8];
#pragma unroll
            for (int j = 0; j < 8; j++) acc[j] = f32x4{0, 0, 0, 0};
            const char* ha = smem + H_HB + (n >> 2) * 144 + g * 16;
            gemv8(f2, lds128(ha), acc);
            gemv8(f3, lds128(ha + 64), acc);
            if (t < 4 || n < 8) {
                const bool up = (n & 4) != 0;
                u32x4 va, vb;
#pragma unroll
                for (int e = 0; e < 4; e++) {
                    const u32 lo = tag12(acc[e][0], tg12), hi = tag12(acc[4 + e][0], tg12);
                    const u32 fr = __builtin_amdgcn_update_dpp(0, hi, 0x114, 0xf, 0xf, true);
                    const u32 fl = __builtin_amdgcn_update_dpp(0, hi, 0x104, 0xf, 0xf, true);
                    va[e] = up ? fr : lo; vb[e] = up ? lo : fl;
                }
                const u32 vo = (u32)(((g * 18 + 4 * t + 2 * (n >> 3)) * 72 + u) * 128 + (n & 3) * 32);
                stb(va, rws, vo + (up ? 16 : 0), WS_YP);
                stb(vb, rws, vo + 72 * 128 + (up ? 0 : 16), WS_YP);
            }
        }
    }
    STAMP(4);
    if (k >= 56) {
        const int r = k - 56, b = r / 18, m0 = (r - b * 18) * 32;
        float xres = 0.f;
        if (tid < 32) xres = lo2f(x[b * 576 + m0 + tid]);
        f32x4 ys = {0, 0, 0, 0}, ss = {0, 0, 0, 0};
        {
            const u32 vo = (u32)(r * 72 * 128 + tid * 16);
            for (int it = 0; it < SPIN_MAX; it++) {
                u32x4 a = ldb<AUX_SC1>(rws, vo, WS_YP);
                u32x4 d = ldb<AUX_SC1>(rws, tid < 64 ? vo + 8192 : OOB, WS_YP);
                u32 bad = 0;
#pragma unroll
                for (int e = 0; e < 4; e++) bad |= (a[e] ^ tg12) | (tid < 64 ? (d[e] ^ tg12) : 0u);
                if (__all((bad & 0xfffu) == 0) || it == SPIN_MAX - 1) {
#pragma unroll
                    for (int e = 0; e < 4; e++) ys[e] = __uint_as_float(a[e] & 0xfffff000u) + __uint_as_float(d[e] & 0xfffff000u);
                    break;
                }
                __builtin_amdgcn_s_sleep(4);
            }
        }
        STAMP(5);
        {
            const u32 vo = (u32)(r * 128 * 128 + tid * 16);
            for (int it = 0; it < SPIN_MAX; it++) {
                u32x4 a = ldb<AUX_SC1>(rws, vo, WS_SP), c = ldb<AUX_SC1>(rws, vo + 8192, WS_SP);
                u32 bad = 0;
#pragma unroll
                for (int e = 0; e < 4; e++) bad |= (a[e] ^ tg12) | (c[e] ^ tg12);
                if (__all((bad & 0xfffu) == 0) || it == SPIN_MAX - 1) {
#pragma unroll
                    for (int e = 0; e < 4; e++) ss[e] = __uint_as_float(a[e] & 0xfffff000u) + __uint_as_float(c[e] & 0xfffff000u);
                    break;
                }
                __builtin_amdgcn_s_sleep(1);
            }
        }
        STAMP(6);
#pragma unroll
        for (int e = 0; e < 4; e++) {
            ys[e] += dpp<0x128>(ys[e]); ss[e] += dpp<0x128>(ss[e]);
            ys[e] = swap32sum(swap16sum(ys[e])); ss[e] = swap32sum(swap16sum(ss[e]));
        }
        float* fin = (float*)(smem + H_FIN);
        if (l < 8) { *(f32x4*)(fin + w * 32 + l * 4) = ys; *(f32x4*)(fin + 256 + w * 32 + l * 4) = ss; }
        bar();
        FLOOR(1);
        if (tid < 32) {
            float y = 0, s = 0;
#pragma unroll
            for (int i = 0; i < 8; i++) { y += fin[i * 32 + tid]; s += fin[256 + i * 32 + tid]; }
            y = rbf(y); s = rbf(s);
            const float gt = rbf(y / (1.0f + __expf(-y)));
            out[b * 576 + m0 + tid] = (u16)pk2(xres + rbf(s * gt), 0.f);
        }
        if (k == 56 && tid == 0) __hip_atomic_store((u32*)ws, T, __ATOMIC_RELAXED, __HIP_MEMORY_SCOPE_AGENT);
        STAMP(7);
    }
    STAMP_FLUSH;
}

#define A_Q 131072
DI void dma_row(const u16* gbase, u32 voff, u32 ldsaddr) {
    u32 keep;
    asm volatile("s_mov_b32 %0, m0\n\ts_mov_b32 m0, %3\n\ts_nop 0\n\tglobal_load_lds_dwordx4 %1, %2" KV_NT "\n\ts_mov_b32 m0, %0"
                 : "=&s"(keep) : "v"(voff), "s"(gbase), "s"(ldsaddr) : "memory");
}
template <int OFF> DI void ld_sc1_asm(u32x4& r, u32 voff, const char* base) {
    asm volatile("global_load_dwordx4 %0, %1, %2 offset:%3 sc1" : "=&v"(r) : "v"(voff), "s"(base), "n"(OFF) : "memory");
}

DI void a_weights(u32x4 (&w0)[8], u32x4 (&w1)[8], u32x4 (&w2)[8], const u16* wuv, const u16* wo, int w, int h, int al, int n, int g) {
    const int kb = w & 3;
    const rsrc_t ro = mkrs(wo, 4096 * 576 * 2);
    const u32 vo = (u32)(8 * g * 1152 + n * 16);
    const u32 so = (u32)((h * 128 + 32 * kb) * 1152);
    if (w < 4) {
        const rsrc_t rv = mkrs(wuv, 512 * 4096 * 2);
        ld8<AUX_WUV>(w0, rv, (u32)(8 * g * 8192 + h * 256 + n * 16), (128 * al + 32 * kb) * 8192, 8192);
    } else {
        ld8<AUX_WO>(w0, ro, vo, so, 1152);
    }
    ld8<AUX_WO>(w1, ro, vo, so + (1 + (w >> 2)) * 256, 1152);
    ld8<AUX_WO>(w2, ro, (w >= 4 && n >= 8) ? OOB : vo, so + (3 + (w >> 2)) * 256, 1152);
}

DI void a_role(char* smem, const u16* ckv, const u16* wuv, const u16* wo, char* ws, u32 pf) {
    const int tid = threadIdx.x, l = tid & 63, w = __builtin_amdgcn_readfirstlane(tid >> 6);
    const int a = blockIdx.x - 128, xc = a & 7, ii = a >> 3;
    const int b = xc >> 1, chunk = (xc & 1) * 16 + ii;
    const int h = 4 * xc + (ii >> 2), al = ii & 3;
    const int n = l & 15, g = l >> 4, hh = l >> 5;
    STAMP_DECL;
    STAMP(0);
    const u32 t_fl = (u32)__builtin_amdgcn_s_memrealtime();
    const rsrc_t rws = mkrs(ws, WS_END);
    u32 E;
    asm volatile("global_load_dword %0, %1, %2 sc1" : "=&v"(E) : "v"(0u), "s"(ws) : "memory");
    {
        const u32x4 ones = {~0u, ~0u, ~0u, ~0u};
#pragma unroll
        for (int i = 0; i < 4; i++) stb(ones, rws, (u32)(tid * 16 + i * 8192), WS_PART + (h * 4 + al) * 32768);
        stb(ones, rws, tid < 16 ? (u32)(tid * 16 + a * 256 + WS_ST) : OOB, 0);
    }
    const u32 lds0 = (u32)(size_t)(LAS char*)smem;
    const u16* kb0 = ckv + ((size_t)b * 4096 + chunk * 128 + 16 * w) * 512;
    const u32 ldsw = lds0 + w * 16384;
#pragma unroll
    for (int i = 0; i < CLR_EARLY; i++) dma_row(kb0, (u32)(((l ^ (((i & 3) << 2) | (i >> 2))) << 4) + i * 1024), ldsw + i * 1024);
    asm volatile("s_waitcnt vmcnt(%1)" : "+v"(E) : "n"(CLR_EARLY), "v"(pf) : "memory");
    const u32 T = E + 1;
    bar();
    if (tid == 0) __builtin_amdgcn_raw_buffer_store_b32(T, rws, (u32)(a * 128 + WS_CL), 0, AUX_SC1);
#pragma unroll
    for (int i = CLR_EARLY; i < 16; i++) dma_row(kb0, (u32)(((l ^ (((i & 3) << 2) | (i >> 2))) << 4) + i * 1024), ldsw + i * 1024);
    u32x4 w0[8], w1[8], w2[8];
    if (w < 4) a_weights(w0, w1, w2, wuv, wo, w, h, al, n, g);
    asm volatile("s_waitcnt vmcnt(0)" ::: "memory");
    {
        const char* qsrc = ws + WS_QA + b * 65536 + w * 8192;
        const u32 vq = l * 16 + 4096;
        const u32 tg = T & 0xffffu;
        u32x4 q[8];
        STAMP(1);
        for (int it = 0; it < SPIN_MAX; it++) {
            ld_sc1_asm<-4096>(q[0], vq, qsrc); ld_sc1_asm<-3072>(q[1], vq, qsrc);
            ld_sc1_asm<-2048>(q[2], vq, qsrc); ld_sc1_asm<-1024>(q[3], vq, qsrc);
            ld_sc1_asm<0>(q[4], vq, qsrc); ld_sc1_asm<1024>(q[5], vq, qsrc);
            ld_sc1_asm<2048>(q[6], vq, qsrc); ld_sc1_asm<3072>(q[7], vq, qsrc);
            asm volatile("s_waitcnt vmcnt(0)" : "+v"(q[0]), "+v"(q[1]), "+v"(q[2]), "+v"(q[3]), "+v"(q[4]), "+v"(q[5]), "+v"(q[6]), "+v"(q[7]) :: "memory");
            u32 bad = 0;
#pragma unroll
            for (int i = 0; i < 8; i++) bad |= (q[i][0] ^ tg) | (q[i][1] ^ tg) | (q[i][2] ^ tg) | (q[i][3] ^ tg);
            if (__all((bad & 0xffffu) == 0)) break;
            __builtin_amdgcn_s_sleep(POLL_SLEEP);
        }
        STAMP(2);
        const int z = (l >> 1) & 7;
        char* qd = smem + A_Q + (8 * (w >> 1) + ((l & 31) >> 2)) * 1024 + ((l >> 1) & 1) * 512 + (l & 1) * 8;
#pragma unroll
        for (int i = 0; i < 8; i++) {
            const int hq = 16 * (w & 1) + 2 * i + hh;
            u32x2 o = {__builtin_amdgcn_perm(q[i][1], q[i][0], 0x07060302u), __builtin_amdgcn_perm(q[i][3], q[i][2], 0x07060302u)};
            *(u32x2*)(qd + ((hq ^ z) << 4)) = o;
        }
    }
    bar();
    FLOOR(3);
    STAMP(3);
    if (w >= 4) { a_weights(w0, w1, w2, wuv, wo, w, h, al, n, g); pin(); }
    f32x16 sacc;
    float mx = 0.f;
    if (w < 4) {
#pragma unroll
        for (int r = 0; r < 16; r++) sacc[r] = 0.f;
        const int row = 32 * w + (l & 31);
        const int f = ((row & 3) << 2) | ((row >> 2) & 3);
        const char* kp = smem + row * 1024;
        const int fx = (f ^ hh) << 4;
        const char* qp = smem + A_Q + hh * 512;
#pragma unroll
        for (int s = 0; s < 32; s++) {
            const u32x4 ka = lds128(kp + ((32 * (s & 7)) ^ fx) + 256 * (s >> 3));
            const u32x4 qb = lds128(qp + s * 1024 + (((l & 31) ^ ((s & 3) * 2 + hh)) << 4));
            sacc = mf32(ka, qb, sacc);
        }
        mx = sacc[0];
#pragma unroll
        for (int r = 1; r < 16; r++) mx = fmaxf(mx, sacc[r]);
        mx = swap32max(mx);
    }
    bar();
    float* aux = (float*)(smem + A_Q);
    if (w < 4 && l < 32) aux[w * 32 + l] = mx;
    bar();
    if (w < 4) {
        const int hq = l & 31;
        const float M = fmaxf(fmaxf(aux[hq], aux[32 + hq]), fmaxf(aux[64 + hq], aux[96 + hq]));
        float p[16], ls = 0.f;
#pragma unroll
        for (int r = 0; r < 16; r++) { p[r] = __builtin_amdgcn_exp2f(sacc[r] - M); ls += p[r]; }
        ls = swap32sum(ls);
        if (l < 32) aux[128 + w * 32 + l] = ls;
        char* pd = smem + A_Q + 1024 + (2 * w) * 1024 + l * 16;
        *(u32x4*)pd = u32x4{pk2(p[0], p[1]), pk2(p[2], p[3]), pk2(p[4], p[5]), pk2(p[6], p[7])};
        *(u32x4*)(pd + 1024) = u32x4{pk2(p[8], p[9]), pk2(p[10], p[11]), pk2(p[12], p[13]), pk2(p[14], p[15])};
    }
    bar();
    STAMP(4);
    if (w == 4 && l < 16) {
        u32x4 o;
#pragma unroll
        for (int e = 0; e < 2; e++) {
            const int hq = 2 * l + e;
            const float M = fmaxf(fmaxf(aux[hq], aux[32 + hq]), fmaxf(aux[64 + hq], aux[96 + hq]));
            const float L = (aux[128 + hq] + aux[160 + hq]) + (aux[192 + hq] + aux[224 + hq]);
            o[2 * e] = min(__float_as_uint(M), 0xfffffffeu); o[2 * e + 1] = min(__float_as_uint(L), 0xfffffffeu);
        }
        stb(o, rws, (u32)((b * 32 + chunk) * 256 + l * 16), WS_ST);
    }
    {
        f32x16 o0, o1;
#pragma unroll
        for (int r = 0; r < 16; r++) { o0[r] = 0.f; o1[r] = 0.f; }
        const int q4 = (l & 15) >> 2, p4 = l & 3, blk = (l >> 4) & 1;
        const char* vb = smem + (4 * hh + q4) * 1024 + (w >> 1) * 256 + (p4 >> 1) * 8;
        const int cl = 8 * (w & 1) + 2 * blk + (p4 & 1);
        const char* pp = smem + A_Q + 1024 + l * 16;
#pragma unroll
        for (int ks = 0; ks < 8; ks++) {
            const u32x4 pb = lds128(pp + ks * 1024);
            u32x4 va[2];
#pragma unroll
            for (int cb = 0; cb < 2; cb++) {
#pragma unroll
                for (int t = 0; t < 2; t++) {
                    const int f = (q4 << 2) | (hh + 2 * t);
                    const char* ad = vb + ks * 16384 + t * 8192 + (((cl + 4 * cb) ^ f) << 4);
                    s16x4 v = __builtin_amdgcn_ds_read_tr16_b64_v4i16((LAS s16x4*)ad);
                    u32x2 vv = __builtin_bit_cast(u32x2, v);
                    va[cb][2 * t] = vv[0]; va[cb][2 * t + 1] = vv[1];
                }
            }
            o0 = mf32(va[0], pb, o0);
            o1 = mf32(va[1], pb, o1);
        }
        STAMP(5);
        u32x4 s0[2], s1[2];
#pragma unroll
        for (int i = 0; i < 2; i++) {
#pragma unroll
            for (int e = 0; e < 4; e++) {
                s0[i][e] = min(pk2(o0[8 * i + 2 * e], o0[8 * i + 2 * e + 1]), 0xfffffffeu);
                s1[i][e] = min(pk2(o1[8 * i + 2 * e], o1[8 * i + 2 * e + 1]), 0xfffffffeu);
            }
        }
        char* stg = smem + A_Q + 9216 + w * 2304;
        char* sw = stg + (l & 15) * 144 + hh * 16;
        const char* sr = stg + (l >> 3) * 144 + (l & 7) * 16;
        const u32 vo = (u32)((((l >> 3) * 4 + (w >> 1)) * 128 + b * 32 + chunk) * 256 + (w & 1) * 128 + (l & 7) * 16);
#pragma unroll
        for (int rd = 0; rd < 2; rd++) {
            if (((l >> 4) & 1) == rd) {
                *(u32x4*)(sw) = s0[0]; *(u32x4*)(sw + 32) = s0[1]; *(u32x4*)(sw + 64) = s1[0]; *(u32x4*)(sw + 96) = s1[1];
            }
            asm volatile("" ::: "memory");
            const u32x4 v0 = lds128(sr), v1 = lds128(sr + 8 * 144);
            stb(v0, rws, vo + (16 * rd) * 131072, WS_PART);
            stb(v1, rws, vo + (16 * rd + 8) * 131072, WS_PART);
            asm volatile("" ::: "memory");
        }
    }
    STAMP(6);
    u32x4 t0[8], t1[8], t2[8];
    tr8(w0, t0); tr8(w1, t1); tr8(w2, t2);
#pragma unroll
    for (int j = 0; j < 8; j++) asm volatile("" : "+v"(t0[j]), "+v"(t1[j]), "+v"(t2[j]));
    STAMP(7);
#define D_WT (A_Q + 28672)
#define D_OB 4096
#define D_CX 8192
#define D_SR 16384
    u32x4 pt[8];
    if (w < 4) {
        const u32 vo = (u32)((w * 32 + 8 * g) * 256 + n * 16);
        const u32 so = WS_PART + (h * 4 + al) * 32768;
        u32x4 pr[8];
        for (int it = 0; it < SPIN_MAX; it++) {
#pragma unroll
            for (int i = 0; i < 8; i++) pr[i] = ldb<AUX_SC1>(rws, vo + i * 256, so);
            u32 mxv = 0;
#pragma unroll
            for (int i = 0; i < 8; i++) mxv = max(max(mxv, max(pr[i][0], pr[i][1])), max(pr[i][2], pr[i][3]));
            if (__all(mxv != 0xffffffffu)) break;
            __builtin_amdgcn_s_sleep(1);
        }
        tr8(pr, pt);
    }
    if (w == 4 || w == 5) {
        const u32 vo = (u32)(((2 * (w - 4) + hh) * 32 + (l & 31)) * 256 + h * 8);
        u32x2 sr;
        for (int it = 0; it < SPIN_MAX; it++) {
            sr = __builtin_amdgcn_raw_buffer_load_b64(rws, vo, WS_ST, AUX_SC1);
            if (__all(max(sr[0], sr[1]) != 0xffffffffu)) break;
            __builtin_amdgcn_s_sleep(1);
        }
        const float m = __uint_as_float(sr[0]);
        float M = m;
        M = fmaxf(M, dpp<0xb1>(M)); M = fmaxf(M, dpp<0x4e>(M)); M = fmaxf(M, dpp<0x141>(M)); M = fmaxf(M, dpp<0x140>(M)); M = swap16max(M);
        const float e = __builtin_amdgcn_exp2f(m - M);
        float L = e * __uint_as_float(sr[1]);
        L += dpp<0xb1>(L); L += dpp<0x4e>(L); L += dpp<0x141>(L); L += dpp<0x140>(L); L = swap16sum(L);
        const float a = e / L;
        const u32 hi = pk2(a, 0.f);
        const u32 lo = pk2(a - lo2f(hi), 0.f);
        u16* wt = (u16*)(smem + D_WT) + (2 * (w - 4) + hh) * 64 + (l & 31);
        wt[0] = (u16)hi; wt[32] = (u16)lo;
    }
    bar();
    FLOOR(4);
    STAMP(8);
    if (w < 4) {
        u32x4 aw = lds128(smem + D_WT + w * 128 + (n & 1) * 64 + g * 16);
        if (n >= 2) aw = u32x4{0, 0, 0, 0};
        float o[8];
#pragma unroll
        for (int j = 0; j < 8; j++) { f32x4 d = mf16(aw, pt[j], f32x4{0, 0, 0, 0}); o[j] = d[0] + d[1]; }
        if (g == 0) *(u32x4*)(smem + D_OB + w * 320 + n * 16) = u32x4{pk2(o[0], o[1]), pk2(o[2], o[3]), pk2(o[4], o[5]), pk2(o[6], o[7])};
    }
    bar();
    if (w < 4) {
        const u32x4 ao = lds128(smem + D_OB + (n >> 2) * 320 + (32 * w + 8 * g) * 2);
        f32x4 d[8];
#pragma unroll
        for (int j = 0; j < 8; j++) d[j] = mf16(ao, t0[j], f32x4{0, 0, 0, 0});
        float* cx = (float*)(smem + D_CX) + (w * 4 + g) * 128 + 8 * n;
        *(f32x4*)cx = f32x4{d[0][0], d[1][0], d[2][0], d[3][0]};
        *(f32x4*)(cx + 4) = f32x4{d[4][0], d[5][0], d[6][0], d[7][0]};
    }
    bar();
    {
        const int kb = w & 3;
        const float* cx = (const float*)(smem + D_CX) + (n >> 2) * 128 + 32 * kb + 8 * g;
        f32x4 lo = *(const f32x4*)cx, hi = *(const f32x4*)(cx + 4);
#pragma unroll
        for (int i = 1; i < 4; i++) { lo += *(const f32x4*)(cx + i * 512); hi += *(const f32x4*)(cx + i * 512 + 4); }
        const u32x4 ac = {pk2(lo[0], lo[1]), pk2(lo[2], lo[3]), pk2(hi[0], hi[1]), pk2(hi[2], hi[3])};
        float* sr = (float*)(smem + D_SR) + (kb * 4 + g) * 640 + 8 * n;
        f32x4 d[8];
        if (w >= 4) {
#pragma unroll
            for (int j = 0; j < 8; j++) d[j] = mf16(ac, t0[j], f32x4{0, 0, 0, 0});
            *(f32x4*)sr = f32x4{d[0][0], d[1][0], d[2][0], d[3][0]};
            *(f32x4*)(sr + 4) = f32x4{d[4][0], d[5][0], d[6][0], d[7][0]};
        }
#pragma unroll
        for (int j = 0; j < 8; j++) d[j] = mf16(ac, t1[j], f32x4{0, 0, 0, 0});
        float* s1 = sr + (1 + (w >> 2)) * 128;
        *(f32x4*)s1 = f32x4{d[0][0], d[1][0], d[2][0], d[3][0]};
        *(f32x4*)(s1 + 4) = f32x4{d[4][0], d[5][0], d[6][0], d[7][0]};
#pragma unroll
        for (int j = 0; j < 8; j++) d[j] = mf16(ac, t2[j], f32x4{0, 0, 0, 0});
        float* s2 = sr + (3 + (w >> 2)) * 128;
        *(f32x4*)s2 = f32x4{d[0][0], d[1][0], d[2][0], d[3][0]};
        *(f32x4*)(s2 + 4) = f32x4{d[4][0], d[5][0], d[6][0], d[7][0]};
    }
    bar();
    {
        const u32 tg12 = T & 0xfffu;
#pragma unroll
        for (int i = 0; i < 2; i++) {
            const int ch = tid + 512 * i;
            if (ch < 576) {
                const int bb = ch / 144, m = (ch - bb * 144) * 4;
                const float* sr = (const float*)(smem + D_SR) + bb * 640 + m;
                f32x4 s = *(const f32x4*)sr;
#pragma unroll
                for (int q = 1; q < 4; q++) s += *(const f32x4*)(sr + q * 2560);
                stb(u32x4{tag12(s[0], tg12), tag12(s[1], tg12), tag12(s[2], tg12), tag12(s[3], tg12)}, rws,
                    (u32)(((bb * 18 + (m >> 5)) * 128 + a) * 128 + (m & 31) * 4), WS_SP);
            }
        }
    }
    STAMP(9);
    STAMP_FLUSH;
}

#define IPF 0
template <int V>
__global__ __launch_bounds__(512, 2) void fused_decode(const u16* x, const u16* ckv, const u16* wq, const u16* wuk, const u16* wuv,
                                                       const u16* wo, const u16* wg, const u16* wu, const u16* wd, u16* out, char* ws) {
    extern __shared__ __attribute__((aligned(16))) char smem[];
    u32 pf = 0;
    if (blockIdx.x < 128) h_role(smem, x, wq, wuk, wg, wu, wd, out, ws, pf);
    else a_role(smem, ckv, wuv, wo, ws, pf);
}

extern "C" void kernel_launch(void* const* d_in, const int* in_sizes, int n_in, void* d_out, int out_size, void* d_ws, size_t ws_size,
                              hipStream_t stream) {
    (void)in_sizes; (void)n_in; (void)out_size; (void)ws_size;
    hipFuncSetAttribute((const void*)fused_decode<0>, hipFuncAttributeMaxDynamicSharedMemorySize, 163840);
    fused_decode<0><<<256, 512, 163840, stream>>>((const u16*)d_in[0], (const u16*)d_in[1], (const u16*)d_in[2], (const u16*)d_in[3],
                                                  (const u16*)d_in[4], (const u16*)d_in[5], (const u16*)d_in[7], (const u16*)d_in[8],
                                                  (const u16*)d_in[9], (u16*)d_out, (char*)d_ws);
}
```

```cpp
#include <hip/hip_runtime.h>
typedef unsigned short u16;
typedef unsigned int u32;
typedef unsigned long long u64;
typedef u32 u32x4 __attribute__((ext_vector_type(4)));
typedef u32 u32x2 __attribute__((ext_vector_type(2)));
typedef float f32x4 __attribute__((ext_vector_type(4)));
typedef float f32x2 __attribute__((ext_vector_type(2)));
typedef float f32x16 __attribute__((ext_vector_type(16)));
typedef __bf16 bf16x8 __attribute__((ext_vector_type(8)));
typedef __bf16 bf16x2 __attribute__((ext_vector_type(2)));
typedef short s16x4 __attribute__((ext_vector_type(4)));
typedef __amdgpu_buffer_rsrc_t rsrc_t;
#define DI __device__ __forceinline__
#define LAS __attribute__((address_space(3)))

#define WS_QA   4096u
#define WS_PART (WS_QA + 262144u)
#define WS_ST   (WS_PART + 4194304u)
#define WS_SP   (WS_ST + 32768u)
#define WS_YP   (WS_SP + 1179648u)
#define WS_CL   (WS_YP + 1327104u)
#define WS_END  (WS_CL + 16384u)
#define WS_DBG  (8u << 20)

#define SPIN_MAX 20000
#define AUX_WQ 0
#define AUX_WUK 0
#define AUX_FFN 2
#define AUX_WUV 2
#define AUX_WO 0
#define KV_NT " nt"
#define DMA_SPLIT 12
#define DMA_WAIT 4
#define ZPRIME 1
#define FFN_DELAY 0
#define A_DELAY 0
#define CLR_EARLY 8
#define POLL_SLEEP 1
#define HORD 1
#define FLOOR_AT 1
#define FLOOR_T 3000
#define FLOOR(id) do { if (FLOOR_AT == id) { while ((u32)__builtin_amdgcn_s_memrealtime() - t_fl < FLOOR_T) __builtin_amdgcn_s_sleep(2); } } while (0)
#define EXPT 2
#define AUX_SC1 16
#define OOB 0x80000000u

#define STAMP_DECL
#define STAMP(i) do { } while (0)
#define STAMP_FLUSH

DI float lo2f(u32 v) { return __uint_as_float(v << 16); }
DI float hi2f(u32 v) { return __uint_as_float(v & 0xffff0000u); }
DI u32 pk2(float lo, float hi) { f32x2 v = {lo, hi}; return __builtin_bit_cast(u32, __builtin_convertvector(v, bf16x2)); }
DI float rbf(float f) { return lo2f(pk2(f, 0.f)); }
DI rsrc_t mkrs(const void* p, u32 bytes) { return __builtin_amdgcn_make_buffer_rsrc((void*)p, 0, (int)bytes, 0x00020000); }
template <int AUX> DI u32x4 ldb(rsrc_t rs, u32 voff, u32 soff) { return __builtin_amdgcn_raw_buffer_load_b128(rs, voff, soff, AUX); }
DI void stb(u32x4 v, rsrc_t rs, u32 voff, u32 soff) { __builtin_amdgcn_raw_buffer_store_b128(v, rs, voff + soff, 0, AUX_SC1); }
DI void bar() { asm volatile("s_waitcnt lgkmcnt(0)\n\ts_barrier" ::: "memory"); }
DI void pin() { asm volatile("" ::: "memory"); }
DI f32x4 mf16(u32x4 a, u32x4 b, f32x4 c) { return __builtin_amdgcn_mfma_f32_16x16x32_bf16(__builtin_bit_cast(bf16x8, a), __builtin_bit_cast(bf16x8, b), c, 0, 0, 0); }
DI f32x16 mf32(u32x4 a, u32x4 b, f32x16 c) { return __builtin_amdgcn_mfma_f32_32x32x16_bf16(__builtin_bit_cast(bf16x8, a), __builtin_bit_cast(bf16x8, b), c, 0, 0, 0); }
DI float swap32max(float v) { auto r = __builtin_amdgcn_permlane32_swap(__float_as_uint(v), __float_as_uint(v), false, false); return fmaxf(__uint_as_float(r[0]), __uint_as_float(r[1])); }
DI float swap32sum(float v) { auto r = __builtin_amdgcn_permlane32_swap(__float_as_uint(v), __float_as_uint(v), false, false); return __uint_as_float(r[0]) + __uint_as_float(r[1]); }
DI float swap16max(float v) { auto r = __builtin_amdgcn_permlane16_swap(__float_as_uint(v), __float_as_uint(v), false, false); return fmaxf(__uint_as_float(r[0]), __uint_as_float(r[1])); }
DI float swap16sum(float v) { auto r = __builtin_amdgcn_permlane16_swap(__float_as_uint(v), __float_as_uint(v), false, false); return __uint_as_float(r[0]) + __uint_as_float(r[1]); }
template <int CTRL> DI float dpp(float v) { return __uint_as_float(__builtin_amdgcn_update_dpp(0, __float_as_uint(v), CTRL, 0xf, 0xf, true)); }
DI float wave_sum(float v) {
    v += dpp<0xb1>(v);
    v += dpp<0x4e>(v);
    v += dpp<0x141>(v);
    v += dpp<0x140>(v);
    v = swap16sum(v);
    return swap32sum(v);
}
DI void tr8(const u32x4 (&r)[8], u32x4 (&t)[8]) {
#pragma unroll
    for (int jj = 0; jj < 4; jj++) {
#pragma unroll
        for (int p = 0; p < 4; p++) {
            t[2 * jj][p] = __builtin_amdgcn_perm(r[2 * p + 1][jj], r[2 * p][jj], 0x05040100u);
            t[2 * jj + 1][p] = __builtin_amdgcn_perm(r[2 * p + 1][jj], r[2 * p][jj], 0x07060302u);
        }
    }
}
template <int AUX> DI void ld8(u32x4 (&r)[8], rsrc_t rs, u32 voff, u32 soff, u32 stride) {
#pragma unroll
    for (int i = 0; i < 8; i++) r[i] = ldb<AUX>(rs, voff, soff + i * stride);
}
DI void gemv8(const u32x4 (&r)[8], u32x4 a, f32x4 (&acc)[8]) {
    u32x4 t[8]; tr8(r, t);
#pragma unroll
    for (int j = 0; j < 8; j++) acc[j] = mf16(a, t[j], acc[j]);
}
DI u32 tag12(float v, u32 tg) { return ((__float_as_uint(v) + 0x800u) & 0xfffff000u) | tg; }
DI u32x4 lds128(const char* p) { return *(const u32x4*)p; }

#define XN_STRIDE 1216
#define H_XN   0
#define H_RED  5120
#define H_QB   (H_RED + 20480)
#define H_HB   (H_QB + 1280)
#define H_FIN  (H_HB + 640)
#define H_ST   (H_FIN + 2048)

DI void h_role(char* smem, const u16* x, const u16* wq, const u16* wuk, const u16* wg, const u16* wu, const u16* wd,
               u16* out, char* ws, u32 pf) {
    const int tid = threadIdx.x, l = tid & 63, w = __builtin_amdgcn_readfirstlane(tid >> 6);
    const int k = blockIdx.x, xc = k & 7, ii = k >> 3, h = 4 * xc + (ii >> 2), cq = ii & 3;
    const int n = l & 15, g = l >> 4;
    STAMP_DECL;
    STAMP(0);
    const u32 t_fl = (u32)__builtin_amdgcn_s_memrealtime();
    const rsrc_t rws = mkrs(ws, WS_END);
    const u32 E = __hip_atomic_load((const u32*)ws, __ATOMIC_RELAXED, __HIP_MEMORY_SCOPE_AGENT);
    u32 ptouch = 0;
    if (EXPT == 1) asm volatile("global_load_dword %0, %1, %2" : "=&v"(ptouch) : "v"(0u), "s"(out) : "memory");
    u32x2 xv[3];
    {
        const rsrc_t rx = mkrs(x, 4 * 576 * 2);
        const u32 vo = (w < 4) ? (u32)(w * 1152 + l * 8) : OOB;
        xv[0] = __builtin_amdgcn_raw_buffer_load_b64(rx, vo, 0, 0);
        xv[1] = __builtin_amdgcn_raw_buffer_load_b64(rx, vo, 512, 0);
        xv[2] = __builtin_amdgcn_raw_buffer_load_b64(rx, l < 16 ? vo : OOB, 1024, 0);
    }
    const rsrc_t rq = mkrs(wq, 576 * 4096 * 2);
    const int rot = (9 * cq) >> 1;
    int b0 = w + rot; if (b0 >= 18) b0 -= 18;
    int b1 = w + 8 + rot; if (b1 >= 18) b1 -= 18;
    int b2 = w + 16 + rot; if (b2 >= 18) b2 -= 18;
    const u32 vq = (u32)(8 * g * 8192 + h * 256 + n * 16);
    u32x4 r0[8], r1[8], r2[8];
    u32x4 uk[4];
    ld8<AUX_WQ>(r0, rq, vq, b0 * 262144, 8192);
    {
        const rsrc_t ru = mkrs(wuk, 512 * 4096 * 2);
        const u32 vo = (u32)(((128 * cq + 16 * w + (n & 7)) * 32 + h) * 256 + ((n >> 3) * 4 + g) * 16);
#pragma unroll
        for (int s = 0; s < 4; s++) uk[s] = ldb<AUX_WUK>(ru, vo + (s & 1) * 128, (s >> 1) * 8 * 8192);
    }
    pin();
    if (w < 4) {
        float v[12];
#pragma unroll
        for (int i = 0; i < 3; i++) { v[4 * i] = lo2f(xv[i][0]); v[4 * i + 1] = hi2f(xv[i][0]); v[4 * i + 2] = lo2f(xv[i][1]); v[4 * i + 3] = hi2f(xv[i][1]); }
        float s = 0;
#pragma unroll
        for (int i = 0; i < 12; i++) s += v[i];
        const float mf = wave_sum(s) * (1.0f / 576);
        const float m = rbf(mf);
        float q = 0;
#pragma unroll
        for (int i = 0; i < 8; i++) { float d = v[i] - mf; q += d * d; }
        if (l < 16) {
#pragma unroll
            for (int i = 8; i < 12; i++) { float d = v[i] - mf; q += d * d; }
        }
        const float var = rbf(wave_sum(q) * (1.0f / 576));
        const float den = rbf(sqrtf(rbf(var + 1e-5f)));
#pragma unroll
        for (int i = 0; i < 3; i++) {
            u32x2 o;
            o[0] = pk2(rbf(v[4 * i] - m) / den, rbf(v[4 * i + 1] - m) / den);
            o[1] = pk2(rbf(v[4 * i + 2] - m) / den, rbf(v[4 * i + 3] - m) / den);
            if (i < 2 || l < 16) *(u32x2*)(smem + H_XN + w * XN_STRIDE + i * 512 + l * 8) = o;
        }
    }
    pin();
    STAMP(8);
    ld8<AUX_WQ>(r1, rq, vq, b1 * 262144, 8192);
    ld8<AUX_WQ>(r2, rq, w < 2 ? vq : OOB, b2 * 262144, 8192);
    STAMP(9);
    u32 clf = __builtin_amdgcn_raw_buffer_load_b32(rws, (u32)(k * 128), WS_CL, AUX_SC1);
    pin();
    bar();
    FLOOR(5);
    STAMP(1);
    {
        f32x4 acc[8];
#pragma unroll
        for (int j = 0; j < 8; j++) acc[j] = f32x4{0, 0, 0, 0};
        const char* xa = smem + H_XN + (n >> 2) * XN_STRIDE + g * 16;
        gemv8(r0, lds128(xa + b0 * 64), acc);
        gemv8(r1, lds128(xa + b1 * 64), acc);
        if (w < 2) gemv8(r2, lds128(xa + b2 * 64), acc);
        float* red = (float*)(smem + H_RED) + (w * 4 + g) * 128 + 4 * n;
        *(f32x4*)red = f32x4{acc[0][0], acc[1][0], acc[2][0], acc[3][0]};
        *(f32x4*)(red + 64) = f32x4{acc[4][0], acc[5][0], acc[6][0], acc[7][0]};
    }
    asm volatile("" :: "v"(pf), "v"(ptouch));
    STAMP(2);
    bar();
    {
        const float* red = (const float*)(smem + H_RED) + tid;
        float s = 0;
#pragma unroll
        for (int i = 0; i < 8; i++) s += red[i * 512];
        const int b = tid >> 7, idx = tid & 127, d = 8 * ((idx >> 2) & 15) + 4 * (idx >> 6) + (idx & 3);
        *(u16*)(smem + H_QB + b * 320 + d * 2) = (u16)pk2(s, 0.f);
    }
    bar();
    const u32 T = E + 1;
    if (EXPT == 3 && k == 0 && tid == 0) __hip_atomic_store((u64*)(ws + 2048), ((u64)T << 32) | t_fl, __ATOMIC_RELAXED, __HIP_MEMORY_SCOPE_AGENT);
    {
        const char* qa = smem + H_QB + (n & 3) * 320 + ((n >> 2) & 1) * 64 + g * 16;
        const u32x4 q0 = lds128(qa), q1 = lds128(qa + 128);
        f32x4 a0 = mf16(uk[0], q0, f32x4{0, 0, 0, 0}), a1 = mf16(uk[2], q0, f32x4{0, 0, 0, 0});
        a0 = mf16(uk[1], q1, a0); a1 = mf16(uk[3], q1, a1);
        if (n < 8 && (n >> 2) == (g >> 1)) {
            float* st = (float*)(smem + H_ST) + (g >> 1) * 512 + (n & 3) * 128 + 16 * w + 4 * (g & 1);
            *(f32x4*)st = a0; *(f32x4*)(st + 8) = a1;
        }
        if (w < 2) {
            for (int it = 0; it < SPIN_MAX && clf != T; it++) { __builtin_amdgcn_s_sleep(1); clf = __builtin_amdgcn_raw_buffer_load_b32(rws, (u32)(k * 128), WS_CL, AUX_SC1); }
        }
        bar();
        FLOOR(2);
        if (w < 2) {
            const float* st = (const float*)(smem + H_ST) + tid * 4;
            const f32x4 v = *(const f32x4*)st + *(const f32x4*)(st + 512);
            const float sc = 0.08838834764831845f * 1.4426950408889634f;
            const u32 tg = T & 0xffffu;
            u32x4 o;
#pragma unroll
            for (int r = 0; r < 4; r++) o[r] = (pk2(0.f, v[r] * sc) & 0xffff0000u) | tg;
            stb(o, rws, (u32)((((tid >> 5) * 4 + cq) * 32 + h) * 512 + (tid & 31) * 16), WS_QA);
        }
    }
    STAMP(3);
    const u32 tg12 = T & 0xfffu;
    if (k >= 56) {
        const int u = k - 56;
        const bool gu3 = (w & 3) == 0, dn = !gu3 && w != 7;
        const int t = w < 4 ? w - 1 : w - 2;
        const rsrc_t rg = mkrs(w < 4 ? wg : wu, 576 * 4608 * 2);
        const rsrc_t rd = mkrs(gu3 ? (w < 4 ? wg : wu) : wd, 576 * 4608 * 2);
        u32x4 f0[8], f1[8], f2[8], f3[8];
        const u32 vg = (u32)(((n >> 3) * 32 + 8 * g) * 9216 + (n & 7) * 16 + u * 128);
        const u32 vd = (!dn || (t == 4 && n >= 8)) ? OOB : (u32)(8 * g * 1152 + n * 16 + t * 256);
        ld8<AUX_FFN>(f0, rg, vg, (w & 3) * 128 * 9216, 9216);
        ld8<AUX_FFN>(f1, rg, vg, ((w & 3) * 128 + 64) * 9216, 9216);
        ld8<AUX_FFN>(f2, rd, gu3 ? vg : vd, gu3 ? 512 * 9216 : u * 64 * 1152, gu3 ? 9216 : 1152);
        ld8<AUX_FFN>(f3, rd, vd, (u * 64 + 32) * 1152, 1152);
        pin();
        STAMP(10);
        const char* xa = smem + H_XN + (n & 3) * XN_STRIDE + (((n >> 2) & 1) * 32 + 8 * g) * 2;
        float* red = (float*)(smem + H_RED);
        {
            f32x4 acc[8];
#pragma unroll
            for (int j = 0; j < 8; j++) acc[j] = f32x4{0, 0, 0, 0};
            gemv8(f0, lds128(xa + (w & 3) * 256), acc);
            gemv8(f1, lds128(xa + (w & 3) * 256 + 128), acc);
            if (gu3) gemv8(f2, lds128(xa + 1024), acc);
            if ((g & 1) == (n >> 3)) {
                float* p = red + ((w * 2 + (n >> 3)) * 4 + (g >> 1) * 2) * 64 + (n & 7) * 8;
                const bool up = g >= 2;
#pragma unroll
                for (int b = 0; b < 2; b++) {
                    *(f32x4*)(p + b * 64) = up ? f32x4{acc[0][2 + b], acc[1][2 + b], acc[2][2 + b], acc[3][2 + b]} : f32x4{acc[0][b], acc[1][b], acc[2][b], acc[3][b]};
                    *(f32x4*)(p + b * 64 + 4) = up ? f32x4{acc[4][2 + b], acc[5][2 + b], acc[6][2 + b], acc[7][2 + b]} : f32x4{acc[4][b], acc[5][b], acc[6][b], acc[7][b]};
                }
            }
        }
        bar();
        STAMP(11);
        if (tid < 256) {
            float gs = 0, us = 0;
#pragma unroll
            for (int i = 0; i < 8; i++) { gs += red[i * 256 + tid]; us += red[(8 + i) * 256 + tid]; }
            const float gg = rbf(gs), uu = rbf(us);
            const float inner = 0.7978845608028654f * (gg + 0.044715f * gg * gg * gg);
            const float ge = rbf(0.5f * gg * (1.0f + tanhf(inner)));
            *(u16*)(smem + H_HB + (tid >> 6) * 144 + (tid & 63) * 2) = (u16)pk2(ge * uu, 0.f);
        }
        bar();
        if (dn) {
            f32x4 acc[8];
#pragma unroll
            for (int j = 0; j < 8; j++) acc[j] = f32x4{0, 0, 0, 0};
            const char* ha = smem + H_HB + (n >> 2) * 144 + g * 16;
            gemv8(f2, lds128(ha), acc);
            gemv8(f3, lds128(ha + 64), acc);
            if (t < 4 || n < 8) {
                const bool up = (n & 4) != 0;
                u32x4 va, vb;
#pragma unroll
                for (int e = 0; e < 4; e++) {
                    const u32 lo = tag12(acc[e][0], tg12), hi = tag12(acc[4 + e][0], tg12);
                    const u32 fr = __builtin_amdgcn_update_dpp(0, hi, 0x114, 0xf, 0xf, true);
                    const u32 fl = __builtin_amdgcn_update_dpp(0, hi, 0x104, 0xf, 0xf, true);
                    va[e] = up ? fr : lo; vb[e] = up ? lo : fl;
                }
                const u32 vo = (u32)(((g * 18 + 4 * t + 2 * (n >> 3)) * 72 + u) * 128 + (n & 3) * 32);
                stb(va, rws, vo + (up ? 16 : 0), WS_YP);
                stb(vb, rws, vo + 72 * 128 + (up ? 0 : 16), WS_YP);
            }
        }
    }
    STAMP(4);
    if (k >= 56) {
        const int r = k - 56, b = r / 18, m0 = (r - b * 18) * 32;
        float xres = 0.f;
        if (tid < 32) xres = lo2f(x[b * 576 + m0 + tid]);
        f32x4 ys = {0, 0, 0, 0}, ss = {0, 0, 0, 0};
        {
            const u32 vo = (u32)(r * 72 * 128 + tid * 16);
            for (int it = 0; it < SPIN_MAX; it++) {
                u32x4 a = ldb<AUX_SC1>(rws, vo, WS_YP);
                u32x4 d = ldb<AUX_SC1>(rws, tid < 64 ? vo + 8192 : OOB, WS_YP);
                u32 bad = 0;
#pragma unroll
                for (int e = 0; e < 4; e++) bad |= (a[e] ^ tg12) | (tid < 64 ? (d[e] ^ tg12) : 0u);
                if (__all((bad & 0xfffu) == 0) || it == SPIN_MAX - 1) {
#pragma unroll
                    for (int e = 0; e < 4; e++) ys[e] = __uint_as_float(a[e] & 0xfffff000u) + __uint_as_float(d[e] & 0xfffff000u);
                    break;
                }
                __builtin_amdgcn_s_sleep(4);
            }
        }
        STAMP(5);
        {
            const u32 vo = (u32)(r * 128 * 128 + tid * 16);
            for (int it = 0; it < SPIN_MAX; it++) {
                u32x4 a = ldb<AUX_SC1>(rws, vo, WS_SP), c = ldb<AUX_SC1>(rws, vo + 8192, WS_SP);
                u32 bad = 0;
#pragma unroll
                for (int e = 0; e < 4; e++) bad |= (a[e] ^ tg12) | (c[e] ^ tg12);
                if (__all((bad & 0xfffu) == 0) || it == SPIN_MAX - 1) {
#pragma unroll
                    for (int e = 0; e < 4; e++) ss[e] = __uint_as_float(a[e] & 0xfffff000u) + __uint_as_float(c[e] & 0xfffff000u);
                    break;
                }
                __builtin_amdgcn_s_sleep(1);
            }
        }
        STAMP(6);
#pragma unroll
        for (int e = 0; e < 4; e++) {
            ys[e] += dpp<0x128>(ys[e]); ss[e] += dpp<0x128>(ss[e]);
            ys[e] = swap32sum(swap16sum(ys[e])); ss[e] = swap32sum(swap16sum(ss[e]));
        }
        float* fin = (float*)(smem + H_FIN);
        if (l < 8) { *(f32x4*)(fin + w * 32 + l * 4) = ys; *(f32x4*)(fin + 256 + w * 32 + l * 4) = ss; }
        bar();
        if (EXPT == 3) {
            u64 rf = 0;
            for (int it = 0; it < SPIN_MAX; it++) { rf = __hip_atomic_load((const u64*)(ws + 2048), __ATOMIC_RELAXED, __HIP_MEMORY_SCOPE_AGENT); if ((u32)(rf >> 32) == T) break; }
            while ((u32)__builtin_amdgcn_s_memrealtime() - (u32)rf < FLOOR_T) __builtin_amdgcn_s_sleep(2);
        } else
        FLOOR(1);
        if (tid < 32) {
            float y = 0, s = 0;
#pragma unroll
            for (int i = 0; i < 8; i++) { y += fin[i * 32 + tid]; s += fin[256 + i * 32 + tid]; }
            y = rbf(y); s = rbf(s);
            const float gt = rbf(y / (1.0f + __expf(-y)));
            if (EXPT == 2) __hip_atomic_store(out + b * 576 + m0 + tid, (u16)pk2(xres + rbf(s * gt), 0.f), __ATOMIC_RELAXED, __HIP_MEMORY_SCOPE_AGENT);
            else out[b * 576 + m0 + tid] = (u16)pk2(xres + rbf(s * gt), 0.f);
        }
        if (k == 56 && tid == 0) __hip_atomic_store((u32*)ws, T, __ATOMIC_RELAXED, __HIP_MEMORY_SCOPE_AGENT);
        STAMP(7);
    }
    STAMP_FLUSH;
}

#define A_Q 131072
DI void dma_row(const u16* gbase, u32 voff, u32 ldsaddr) {
    u32 keep;
    asm volatile("s_mov_b32 %0, m0\n\ts_mov_b32 m0, %3\n\ts_nop 0\n\tglobal_load_lds_dwordx4 %1, %2" KV_NT "\n\ts_mov_b32 m0, %0"
                 : "=&s"(keep) : "v"(voff), "s"(gbase), "s"(ldsaddr) : "memory");
}
template <int OFF> DI void ld_sc1_asm(u32x4& r, u32 voff, const char* base) {
    asm volatile("global_load_dwordx4 %0, %1, %2 offset:%3 sc1" : "=&v"(r) : "v"(voff), "s"(base), "n"(OFF) : "memory");
}

DI void a_weights(u32x4 (&w0)[8], u32x4 (&w1)[8], u32x4 (&w2)[8], const u16* wuv, const u16* wo, int w, int h, int al, int n, int g) {
    const int kb = w & 3;
    const rsrc_t ro = mkrs(wo, 4096 * 576 * 2);
    const u32 vo = (u32)(8 * g * 1152 + n * 16);
    const u32 so = (u32)((h * 128 + 32 * kb) * 1152);
    if (w < 4) {
        const rsrc_t rv = mkrs(wuv, 512 * 4096 * 2);
        ld8<AUX_WUV>(w0, rv, (u32)(8 * g * 8192 + h * 256 + n * 16), (128 * al + 32 * kb) * 8192, 8192);
    } else {
        ld8<AUX_WO>(w0, ro, vo, so, 1152);
    }
    ld8<AUX_WO>(w1, ro, vo, so + (1 + (w >> 2)) * 256, 1152);
    ld8<AUX_WO>(w2, ro, (w >= 4 && n >= 8) ? OOB : vo, so + (3 + (w >> 2)) * 256, 1152);
}

DI void a_role(char* smem, const u16* ckv, const u16* wuv, const u16* wo, char* ws, u32 pf) {
    const int tid = threadIdx.x, l = tid & 63, w = __builtin_amdgcn_readfirstlane(tid >> 6);
    const int a = blockIdx.x - 128, xc = a & 7, ii = a >> 3;
    const int b = xc >> 1, chunk = (xc & 1) * 16 + ii;
    const int h = 4 * xc + (ii >> 2), al = ii & 3;
    const int n = l & 15, g = l >> 4, hh = l >> 5;
    STAMP_DECL;
    STAMP(0);
    const u32 t_fl = (u32)__builtin_amdgcn_s_memrealtime();
    const rsrc_t rws = mkrs(ws, WS_END);
    u32 E;
    asm volatile("global_load_dword %0, %1, %2 sc1" : "=&v"(E) : "v"(0u), "s"(ws) : "memory");
    {
        const u32x4 ones = {~0u, ~0u, ~0u, ~0u};
#pragma unroll
        for (int i = 0; i < 4; i++) stb(ones, rws, (u32)(tid * 16 + i * 8192), WS_PART + (h * 4 + al) * 32768);
        stb(ones, rws, tid < 16 ? (u32)(tid * 16 + a * 256 + WS_ST) : OOB, 0);
    }
    const u32 lds0 = (u32)(size_t)(LAS char*)smem;
    const u16* kb0 = ckv + ((size_t)b * 4096 + chunk * 128 + 16 * w) * 512;
    const u32 ldsw = lds0 + w * 16384;
#pragma unroll
    for (int i = 0; i < CLR_EARLY; i++) dma_row(kb0, (u32)(((l ^ (((i & 3) << 2) | (i >> 2))) << 4) + i * 1024), ldsw + i * 1024);
    asm volatile("s_waitcnt vmcnt(%1)" : "+v"(E) : "n"(CLR_EARLY), "v"(pf) : "memory");
    const u32 T = E + 1;
    bar();
    if (tid == 0) __builtin_amdgcn_raw_buffer_store_b32(T, rws, (u32)(a * 128 + WS_CL), 0, AUX_SC1);
#pragma unroll
    for (int i = CLR_EARLY; i < 16; i++) dma_row(kb0, (u32)(((l ^ (((i & 3) << 2) | (i >> 2))) << 4) + i * 1024), ldsw + i * 1024);
    u32x4 w0[8], w1[8], w2[8];
    if (w < 4) a_weights(w0, w1, w2, wuv, wo, w, h, al, n, g);
    asm volatile("s_waitcnt vmcnt(0)" ::: "memory");
    {
        const char* qsrc = ws + WS_QA + b * 65536 + w * 8192;
        const u32 vq = l * 16 + 4096;
        const u32 tg = T & 0xffffu;
        u32x4 q[8];
        STAMP(1);
        for (int it = 0; it < SPIN_MAX; it++) {
            ld_sc1_asm<-4096>(q[0], vq, qsrc); ld_sc1_asm<-3072>(q[1], vq, qsrc);
            ld_sc1_asm<-2048>(q[2], vq, qsrc); ld_sc1_asm<-1024>(q[3], vq, qsrc);
            ld_sc1_asm<0>(q[4], vq, qsrc); ld_sc1_asm<1024>(q[5], vq, qsrc);
            ld_sc1_asm<2048>(q[6], vq, qsrc); ld_sc1_asm<3072>(q[7], vq, qsrc);
            asm volatile("s_waitcnt vmcnt(0)" : "+v"(q[0]), "+v"(q[1]), "+v"(q[2]), "+v"(q[3]), "+v"(q[4]), "+v"(q[5]), "+v"(q[6]), "+v"(q[7]) :: "memory");
            u32 bad = 0;
#pragma unroll
            for (int i = 0; i < 8; i++) bad |= (q[i][0] ^ tg) | (q[i][1] ^ tg) | (q[i][2] ^ tg) | (q[i][3] ^ tg);
            if (__all((bad & 0xffffu) == 0)) break;
            __builtin_amdgcn_s_sleep(POLL_SLEEP);
        }
        STAMP(2);
        const int z = (l >> 1) & 7;
        char* qd = smem + A_Q + (8 * (w >> 1) + ((l & 31) >> 2)) * 1024 + ((l >> 1) & 1) * 512 + (l & 1) * 8;
#pragma unroll
        for (int i = 0; i < 8; i++) {
            const int hq = 16 * (w & 1) + 2 * i + hh;
            u32x2 o = {__builtin_amdgcn_perm(q[i][1], q[i][0], 0x07060302u), __builtin_amdgcn_perm(q[i][3], q[i][2], 0x07060302u)};
            *(u32x2*)(qd + ((hq ^ z) << 4)) = o;
        }
    }
    bar();
    FLOOR(3);
    STAMP(3);
    if (w >= 4) { a_weights(w0, w1, w2, wuv, wo, w, h, al, n, g); pin(); }
    f32x16 sacc;
    float mx = 0.f;
    if (w < 4) {
#pragma unroll
        for (int r = 0; r < 16; r++) sacc[r] = 0.f;
        const int row = 32 * w + (l & 31);
        const int f = ((row & 3) << 2) | ((row >> 2) & 3);
        const char* kp = smem + row * 1024;
        const int fx = (f ^ hh) << 4;
        const char* qp = smem + A_Q + hh * 512;
#pragma unroll
        for (int s = 0; s < 32; s++) {
            const u32x4 ka = lds128(kp + ((32 * (s & 7)) ^ fx) + 256 * (s >> 3));
            const u32x4 qb = lds128(qp + s * 1024 + (((l & 31) ^ ((s & 3) * 2 + hh)) << 4));
            sacc = mf32(ka, qb, sacc);
        }
        mx = sacc[0];
#pragma unroll
        for (int r = 1; r < 16; r++) mx = fmaxf(mx, sacc[r]);
        mx = swap32max(mx);
    }
    bar();
    float* aux = (float*)(smem + A_Q);
    if (w < 4 && l < 32) aux[w * 32 + l] = mx;
    bar();
    if (w < 4) {
        const int hq = l & 31;
        const float M = fmaxf(fmaxf(aux[hq], aux[32 + hq]), fmaxf(aux[64 + hq], aux[96 + hq]));
        float p[16], ls = 0.f;
#pragma unroll
        for (int r = 0; r < 16; r++) { p[r] = __builtin_amdgcn_exp2f(sacc[r] - M); ls += p[r]; }
        ls = swap32sum(ls);
        if (l < 32) aux[128 + w * 32 + l] = ls;
        char* pd = smem + A_Q + 1024 + (2 * w) * 1024 + l * 16;
        *(u32x4*)pd = u32x4{pk2(p[0], p[1]), pk2(p[2], p[3]), pk2(p[4], p[5]), pk2(p[6], p[7])};
        *(u32x4*)(pd + 1024) = u32x4{pk2(p[8], p[9]), pk2(p[10], p[11]), pk2(p[12], p[13]), pk2(p[14], p[15])};
    }
    bar();
    STAMP(4);
    if (w == 4 && l < 16) {
        u32x4 o;
#pragma unroll
        for (int e = 0; e < 2; e++) {
            const int hq = 2 * l + e;
            const float M = fmaxf(fmaxf(aux[hq], aux[32 + hq]), fmaxf(aux[64 + hq], aux[96 + hq]));
            const float L = (aux[128 + hq] + aux[160 + hq]) + (aux[192 + hq] + aux[224 + hq]);
            o[2 * e] = min(__float_as_uint(M), 0xfffffffeu); o[2 * e + 1] = min(__float_as_uint(L), 0xfffffffeu);
        }
        stb(o, rws, (u32)((b * 32 + chunk) * 256 + l * 16), WS_ST);
    }
    {
        f32x16 o0, o1;
#pragma unroll
        for (int r = 0; r < 16; r++) { o0[r] = 0.f; o1[r] = 0.f; }
        const int q4 = (l & 15) >> 2, p4 = l & 3, blk = (l >> 4) & 1;
        const char* vb = smem + (4 * hh + q4) * 1024 + (w >> 1) * 256 + (p4 >> 1) * 8;
        const int cl = 8 * (w & 1) + 2 * blk + (p4 & 1);
        const char* pp = smem + A_Q + 1024 + l * 16;
#pragma unroll
        for (int ks = 0; ks < 8; ks++) {
            const u32x4 pb = lds128(pp + ks * 1024);
            u32x4 va[2];
#pragma unroll
            for (int cb = 0; cb < 2; cb++) {
#pragma unroll
                for (int t = 0; t < 2; t++) {
                    const int f = (q4 << 2) | (hh + 2 * t);
                    const char* ad = vb + ks * 16384 + t * 8192 + (((cl + 4 * cb) ^ f) << 4);
                    s16x4 v = __builtin_amdgcn_ds_read_tr16_b64_v4i16((LAS s16x4*)ad);
                    u32x2 vv = __builtin_bit_cast(u32x2, v);
                    va[cb][2 * t] = vv[0]; va[cb][2 * t + 1] = vv[1];
                }
            }
            o0 = mf32(va[0], pb, o0);
            o1 = mf32(va[1], pb, o1);
        }
        STAMP(5);
        u32x4 s0[2], s1[2];
#pragma unroll
        for (int i = 0; i < 2; i++) {
#pragma unroll
            for (int e = 0; e < 4; e++) {
                s0[i][e] = min(pk2(o0[8 * i + 2 * e], o0[8 * i + 2 * e + 1]), 0xfffffffeu);
                s1[i][e] = min(pk2(o1[8 * i + 2 * e], o1[8 * i + 2 * e + 1]), 0xfffffffeu);
            }
        }
        char* stg = smem + A_Q + 9216 + w * 2304;
        char* sw = stg + (l & 15) * 144 + hh * 16;
        const char* sr = stg + (l >> 3) * 144 + (l & 7) * 16;
        const u32 vo = (u32)((((l >> 3) * 4 + (w >> 1)) * 128 + b * 32 + chunk) * 256 + (w & 1) * 128 + (l & 7) * 16);
#pragma unroll
        for (int rd = 0; rd < 2; rd++) {
            if (((l >> 4) & 1) == rd) {
                *(u32x4*)(sw) = s0[0]; *(u32x4*)(sw + 32) = s0[1]; *(u32x4*)(sw + 64) = s1[0]; *(u32x4*)(sw + 96) = s1[1];
            }
            asm volatile("" ::: "memory");
            const u32x4 v0 = lds128(sr), v1 = lds128(sr + 8 * 144);
            stb(v0, rws, vo + (16 * rd) * 131072, WS_PART);
            stb(v1, rws, vo + (16 * rd + 8) * 131072, WS_PART);
            asm volatile("" ::: "memory");
        }
    }
    STAMP(6);
    u32x4 t0[8], t1[8], t2[8];
    tr8(w0, t0); tr8(w1, t1); tr8(w2, t2);
#pragma unroll
    for (int j = 0; j < 8; j++) asm volatile("" : "+v"(t0[j]), "+v"(t1[j]), "+v"(t2[j]));
    STAMP(7);
#define D_WT (A_Q + 28672)
#define D_OB 4096
#define D_CX 8192
#define D_SR 16384
    u32x4 pt[8];
    if (w < 4) {
        const u32 vo = (u32)((w * 32 + 8 * g) * 256 + n * 16);
        const u32 so = WS_PART + (h * 4 + al) * 32768;
        u32x4 pr[8];
        for (int it = 0; it < SPIN_MAX; it++) {
#pragma unroll
            for (int i = 0; i < 8; i++) pr[i] = ldb<AUX_SC1>(rws, vo + i * 256, so);
            u32 mxv = 0;
#pragma unroll
            for (int i = 0; i < 8; i++) mxv = max(max(mxv, max(pr[i][0], pr[i][1])), max(pr[i][2], pr[i][3]));
            if (__all(mxv != 0xffffffffu)) break;
            __builtin_amdgcn_s_sleep(1);
        }
        tr8(pr, pt);
    }
    if (w == 4 || w == 5) {
        const u32 vo = (u32)(((2 * (w - 4) + hh) * 32 + (l & 31)) * 256 + h * 8);
        u32x2 sr;
        for (int it = 0; it < SPIN_MAX; it++) {
            sr = __builtin_amdgcn_raw_buffer_load_b64(rws, vo, WS_ST, AUX_SC1);
            if (__all(max(sr[0], sr[1]) != 0xffffffffu)) break;
            __builtin_amdgcn_s_sleep(1);
        }
        const float m = __uint_as_float(sr[0]);
        float M = m;
        M = fmaxf(M, dpp<0xb1>(M)); M = fmaxf(M, dpp<0x4e>(M)); M = fmaxf(M, dpp<0x141>(M)); M = fmaxf(M, dpp<0x140>(M)); M = swap16max(M);
        const float e = __builtin_amdgcn_exp2f(m - M);
        float L = e * __uint_as_float(sr[1]);
        L += dpp<0xb1>(L); L += dpp<0x4e>(L); L += dpp<0x141>(L); L += dpp<0x140>(L); L = swap16sum(L);
        const float a = e / L;
        const u32 hi = pk2(a, 0.f);
        const u32 lo = pk2(a - lo2f(hi), 0.f);
        u16* wt = (u16*)(smem + D_WT) + (2 * (w - 4) + hh) * 64 + (l & 31);
        wt[0] = (u16)hi; wt[32] = (u16)lo;
    }
    bar();
    FLOOR(4);
    STAMP(8);
    if (w < 4) {
        u32x4 aw = lds128(smem + D_WT + w * 128 + (n & 1) * 64 + g * 16);
        if (n >= 2) aw = u32x4{0, 0, 0, 0};
        float o[8];
#pragma unroll
        for (int j = 0; j < 8; j++) { f32x4 d = mf16(aw, pt[j], f32x4{0, 0, 0, 0}); o[j] = d[0] + d[1]; }
        if (g == 0) *(u32x4*)(smem + D_OB + w * 320 + n * 16) = u32x4{pk2(o[0], o[1]), pk2(o[2], o[3]), pk2(o[4], o[5]), pk2(o[6], o[7])};
    }
    bar();
    if (w < 4) {
        const u32x4 ao = lds128(smem + D_OB + (n >> 2) * 320 + (32 * w + 8 * g) * 2);
        f32x4 d[8];
#pragma unroll
        for (int j = 0; j < 8; j++) d[j] = mf16(ao, t0[j], f32x4{0, 0, 0, 0});
        float* cx = (float*)(smem + D_CX) + (w * 4 + g) * 128 + 8 * n;
        *(f32x4*)cx = f32x4{d[0][0], d[1][0], d[2][0], d[3][0]};
        *(f32x4*)(cx + 4) = f32x4{d[4][0], d[5][0], d[6][0], d[7][0]};
    }
    bar();
    {
        const int kb = w & 3;
        const float* cx = (const float*)(smem + D_CX) + (n >> 2) * 128 + 32 * kb + 8 * g;
        f32x4 lo = *(const f32x4*)cx, hi = *(const f32x4*)(cx + 4);
#pragma unroll
        for (int i = 1; i < 4; i++) { lo += *(const f32x4*)(cx + i * 512); hi += *(const f32x4*)(cx + i * 512 + 4); }
        const u32x4 ac = {pk2(lo[0], lo[1]), pk2(lo[2], lo[3]), pk2(hi[0], hi[1]), pk2(hi[2], hi[3])};
        float* sr = (float*)(smem + D_SR) + (kb * 4 + g) * 640 + 8 * n;
        f32x4 d[8];
        if (w >= 4) {
#pragma unroll
            for (int j = 0; j < 8; j++) d[j] = mf16(ac, t0[j], f32x4{0, 0, 0, 0});
            *(f32x4*)sr = f32x4{d[0][0], d[1][0], d[2][0], d[3][0]};
            *(f32x4*)(sr + 4) = f32x4{d[4][0], d[5][0], d[6][0], d[7][0]};
        }
#pragma unroll
        for (int j = 0; j < 8; j++) d[j] = mf16(ac, t1[j], f32x4{0, 0, 0, 0});
        float* s1 = sr + (1 + (w >> 2)) * 128;
        *(f32x4*)s1 = f32x4{d[0][0], d[1][0], d[2][0], d[3][0]};
        *(f32x4*)(s1 + 4) = f32x4{d[4][0], d[5][0], d[6][0], d[7][0]};
#pragma unroll
        for (int j = 0; j < 8; j++) d[j] = mf16(ac, t2[j], f32x4{0, 0, 0, 0});
        float* s2 = sr + (3 + (w >> 2)) * 128;
        *(f32x4*)s2 = f32x4{d[0][0], d[1][0], d[2][0], d[3][0]};
        *(f32x4*)(s2 + 4) = f32x4{d[4][0], d[5][0], d[6][0], d[7][0]};
    }
    bar();
    {
        const u32 tg12 = T & 0xfffu;
#pragma unroll
        for (int i = 0; i < 2; i++) {
            const int ch = tid + 512 * i;
            if (ch < 576) {
                const int bb = ch / 144, m = (ch - bb * 144) * 4;
                const float* sr = (const float*)(smem + D_SR) + bb * 640 + m;
                f32x4 s = *(const f32x4*)sr;
#pragma unroll
                for (int q = 1; q < 4; q++) s += *(const f32x4*)(sr + q * 2560);
                stb(u32x4{tag12(s[0], tg12), tag12(s[1], tg12), tag12(s[2], tg12), tag12(s[3], tg12)}, rws,
                    (u32)(((bb * 18 + (m >> 5)) * 128 + a) * 128 + (m & 31) * 4), WS_SP);
            }
        }
    }
    STAMP(9);
    STAMP_FLUSH;
}

#define IPF 0
template <int V>
__global__ __launch_bounds__(512, 2) void fused_decode(const u16* x, const u16* ckv, const u16* wq, const u16* wuk, const u16* wuv,
                                                       const u16* wo, const u16* wg, const u16* wu, const u16* wd, u16* out, char* ws) {
    extern __shared__ __attribute__((aligned(16))) char smem[];
    u32 pf = 0;
    if (blockIdx.x < 128) h_role(smem, x, wq, wuk, wg, wu, wd, out, ws, pf);
    else a_role(smem, ckv, wuv, wo, ws, pf);
}

extern "C" void kernel_launch(void* const* d_in, const int* in_sizes, int n_in, void* d_out, int out_size, void* d_ws, size_t ws_size,
                              hipStream_t stream) {
    (void)in_sizes; (void)n_in; (void)out_size; (void)ws_size;
    hipFuncSetAttribute((const void*)fused_decode<0>, hipFuncAttributeMaxDynamicSharedMemorySize, 163840);
    fused_decode<0><<<256, 512, 163840, stream>>>((const u16*)d_in[0], (const u16*)d_in[1], (const u16*)d_in[2], (const u16*)d_in[3],
                                                  (const u16*)d_in[4], (const u16*)d_in[5], (const u16*)d_in[7], (const u16*)d_in[8],
                                                  (const u16*)d_in[9], (u16*)d_out, (char*)d_ws);
}
```

```cpp
#include <hip/hip_runtime.h>
typedef unsigned short u16;
typedef unsigned int u32;
typedef unsigned long long u64;
typedef u32 u32x4 __attribute__((ext_vector_type(4)));
typedef u32 u32x2 __attribute__((ext_vector_type(2)));
typedef float f32x4 __attribute__((ext_vector_type(4)));
typedef float f32x2 __attribute__((ext_vector_type(2)));
typedef float f32x16 __attribute__((ext_vector_type(16)));
typedef __bf16 bf16x8 __attribute__((ext_vector_type(8)));
typedef __bf16 bf16x2 __attribute__((ext_vector_type(2)));
typedef short s16x4 __attribute__((ext_vector_type(4)));
typedef __amdgpu_buffer_rsrc_t rsrc_t;
#define DI __device__ __forceinline__
#define LAS __attribute__((address_space(3)))

#define WS_QA   4096u
#define WS_PART (WS_QA + 262144u)
#define WS_ST   (WS_PART + 4194304u)
#define WS_SP   (WS_ST + 32768u)
#define WS_YP   (WS_SP + 1179648u)
#define WS_CL   (WS_YP + 1327104u)
#define WS_END  (WS_CL + 16384u)
#define WS_DBG  (8u << 20)

#define SPIN_MAX 20000
#define AUX_WQ 0
#define AUX_WUK 0
#define AUX_FFN 2
#define AUX_WUV 2
#define AUX_WO 0
#define KV_NT " nt"
#define DMA_SPLIT 12
#define DMA_WAIT 4
#define ZPRIME 1
#define FFN_DELAY 0
#define A_DELAY 0
#define CLR_EARLY 8
#define POLL_SLEEP 1
#define HORD 1
#define FLOOR_AT 0
#define FLOOR_T 0
#define FLOOR(id) do { if (FLOOR_AT == id) { while ((u32)__builtin_amdgcn_s_memrealtime() - t_fl < FLOOR_T) __builtin_amdgcn_s_sleep(2); } } while (0)
#define EXPT 2
#define DW_EARLY 1
#define AUX_SC1 16
#define OOB 0x80000000u

#define STAMP_DECL
#define STAMP(i) do { } while (0)
#define STAMP_FLUSH

DI float lo2f(u32 v) { return __uint_as_float(v << 16); }
DI float hi2f(u32 v) { return __uint_as_float(v & 0xffff0000u); }
DI u32 pk2(float lo, float hi) { f32x2 v = {lo, hi}; return __builtin_bit_cast(u32, __builtin_convertvector(v, bf16x2)); }
DI float rbf(float f) { return lo2f(pk2(f, 0.f)); }
DI rsrc_t mkrs(const void* p, u32 bytes) { return __builtin_amdgcn_make_buffer_rsrc((void*)p, 0, (int)bytes, 0x00020000); }
template <int AUX> DI u32x4 ldb(rsrc_t rs, u32 voff, u32 soff) { return __builtin_amdgcn_raw_buffer_load_b128(rs, voff, soff, AUX); }
DI void stb(u32x4 v, rsrc_t rs, u32 voff, u32 soff) { __builtin_amdgcn_raw_buffer_store_b128(v, rs, voff + soff, 0, AUX_SC1); }
DI void bar() { asm volatile("s_waitcnt lgkmcnt(0)\n\ts_barrier" ::: "memory"); }
DI void pin() { asm volatile("" ::: "memory"); }
DI f32x4 mf16(u32x4 a, u32x4 b, f32x4 c) { return __builtin_amdgcn_mfma_f32_16x16x32_bf16(__builtin_bit_cast(bf16x8, a), __builtin_bit_cast(bf16x8, b), c, 0, 0, 0); }
DI f32x16 mf32(u32x4 a, u32x4 b, f32x16 c) { return __builtin_amdgcn_mfma_f32_32x32x16_bf16(__builtin_bit_cast(bf16x8, a), __builtin_bit_cast(bf16x8, b), c, 0, 0, 0); }
DI float swap32max(float v) { auto r = __builtin_amdgcn_permlane32_swap(__float_as_uint(v), __float_as_uint(v), false, false); return fmaxf(__uint_as_float(r[0]), __uint_as_float(r[1])); }
DI float swap32sum(float v) { auto r = __builtin_amdgcn_permlane32_swap(__float_as_uint(v), __float_as_uint(v), false, false); return __uint_as_float(r[0]) + __uint_as_float(r[1]); }
DI float swap16max(float v) { auto r = __builtin_amdgcn_permlane16_swap(__float_as_uint(v), __float_as_uint(v), false, false); return fmaxf(__uint_as_float(r[0]), __uint_as_float(r[1])); }
DI float swap16sum(float v) { auto r = __builtin_amdgcn_permlane16_swap(__float_as_uint(v), __float_as_uint(v), false, false); return __uint_as_float(r[0]) + __uint_as_float(r[1]); }
template <int CTRL> DI float dpp(float v) { return __uint_as_float(__builtin_amdgcn_update_dpp(0, __float_as_uint(v), CTRL, 0xf, 0xf, true)); }
DI float wave_sum(float v) {
    v += dpp<0xb1>(v);
    v += dpp<0x4e>(v);
    v += dpp<0x141>(v);
    v += dpp<0x140>(v);
    v = swap16sum(v);
    return swap32sum(v);
}
DI void tr8(const u32x4 (&r)[8], u32x4 (&t)[8]) {
#pragma unroll
    for (int jj = 0; jj < 4; jj++) {
#pragma unroll
        for (int p = 0; p < 4; p++) {
            t[2 * jj][p] = __builtin_amdgcn_perm(r[2 * p + 1][jj], r[2 * p][jj], 0x05040100u);
            t[2 * jj + 1][p] = __builtin_amdgcn_perm(r[2 * p + 1][jj], r[2 * p][jj], 0x07060302u);
        }
    }
}
template <int AUX> DI void ld8(u32x4 (&r)[8], rsrc_t rs, u32 voff, u32 soff, u32 stride) {
#pragma unroll
    for (int i = 0; i < 8; i++) r[i] = ldb<AUX>(rs, voff, soff + i * stride);
}
DI void gemv8(const u32x4 (&r)[8], u32x4 a, f32x4 (&acc)[8]) {
    u32x4 t[8]; tr8(r, t);
#pragma unroll
    for (int j = 0; j < 8; j++) acc[j] = mf16(a, t[j], acc[j]);
}
DI u32 tag12(float v, u32 tg) { return ((__float_as_uint(v) + 0x800u) & 0xfffff000u) | tg; }
DI u32x4 lds128(const char* p) { return *(const u32x4*)p; }

#define XN_STRIDE 1216
#define H_XN   0
#define H_RED  5120
#define H_QB   (H_RED + 20480)
#define H_HB   (H_QB + 1280)
#define H_FIN  (H_HB + 640)
#define H_ST   (H_FIN + 2048)

DI void h_role(char* smem, const u16* x, const u16* wq, const u16* wuk, const u16* wg, const u16* wu, const u16* wd,
               u16* out, char* ws, u32 pf) {
    const int tid = threadIdx.x, l = tid & 63, w = __builtin_amdgcn_readfirstlane(tid >> 6);
    const int k = blockIdx.x, xc = k & 7, ii = k >> 3, h = 4 * xc + (ii >> 2), cq = ii & 3;
    const int n = l & 15, g = l >> 4;
    STAMP_DECL;
    STAMP(0);
    const u32 t_fl = (u32)__builtin_amdgcn_s_memrealtime();
    const rsrc_t rws = mkrs(ws, WS_END);
    u32 E;
    asm volatile("s_load_dword %0, %1, 0x0" : "=s"(E) : "s"(ws) : "memory");
    u32 ptouch = 0;
    if (EXPT == 1) asm volatile("global_load_dword %0, %1, %2" : "=&v"(ptouch) : "v"(0u), "s"(out) : "memory");
    u32x2 xv[3];
    {
        const rsrc_t rx = mkrs(x, 4 * 576 * 2);
        const u32 vo = (w < 4) ? (u32)(w * 1152 + l * 8) : OOB;
        xv[0] = __builtin_amdgcn_raw_buffer_load_b64(rx, vo, 0, 0);
        xv[1] = __builtin_amdgcn_raw_buffer_load_b64(rx, vo, 512, 0);
        xv[2] = __builtin_amdgcn_raw_buffer_load_b64(rx, l < 16 ? vo : OOB, 1024, 0);
    }
    const rsrc_t rq = mkrs(wq, 576 * 4096 * 2);
    const int rot = (9 * cq) >> 1;
    int b0 = w + rot; if (b0 >= 18) b0 -= 18;
    int b1 = w + 8 + rot; if (b1 >= 18) b1 -= 18;
    int b2 = w + 16 + rot; if (b2 >= 18) b2 -= 18;
    const u32 vq = (u32)(8 * g * 8192 + h * 256 + n * 16);
    u32x4 r0[8], r1[8], r2[8];
    u32x4 uk[4];
    ld8<AUX_WQ>(r0, rq, vq, b0 * 262144, 8192);
    {
        const rsrc_t ru = mkrs(wuk, 512 * 4096 * 2);
        const u32 vo = (u32)(((128 * cq + 16 * w + (n & 7)) * 32 + h) * 256 + ((n >> 3) * 4 + g) * 16);
#pragma unroll
        for (int s = 0; s < 4; s++) uk[s] = ldb<AUX_WUK>(ru, vo + (s & 1) * 128, (s >> 1) * 8 * 8192);
    }
    pin();
    if (w < 4) {
        float v[12];
#pragma unroll
        for (int i = 0; i < 3; i++) { v[4 * i] = lo2f(xv[i][0]); v[4 * i + 1] = hi2f(xv[i][0]); v[4 * i + 2] = lo2f(xv[i][1]); v[4 * i + 3] = hi2f(xv[i][1]); }
        float s = 0;
#pragma unroll
        for (int i = 0; i < 12; i++) s += v[i];
        const float mf = wave_sum(s) * (1.0f / 576);
        const float m = rbf(mf);
        float q = 0;
#pragma unroll
        for (int i = 0; i < 8; i++) { float d = v[i] - mf; q += d * d; }
        if (l < 16) {
#pragma unroll
            for (int i = 8; i < 12; i++) { float d = v[i] - mf; q += d * d; }
        }
        const float var = rbf(wave_sum(q) * (1.0f / 576));
        const float den = rbf(sqrtf(rbf(var + 1e-5f)));
#pragma unroll
        for (int i = 0; i < 3; i++) {
            u32x2 o;
            o[0] = pk2(rbf(v[4 * i] - m) / den, rbf(v[4 * i + 1] - m) / den);
            o[1] = pk2(rbf(v[4 * i + 2] - m) / den, rbf(v[4 * i + 3] - m) / den);
            if (i < 2 || l < 16) *(u32x2*)(smem + H_XN + w * XN_STRIDE + i * 512 + l * 8) = o;
        }
    }
    pin();
    STAMP(8);
    ld8<AUX_WQ>(r1, rq, vq, b1 * 262144, 8192);
    ld8<AUX_WQ>(r2, rq, w < 2 ? vq : OOB, b2 * 262144, 8192);
    STAMP(9);
    u32 clf = __builtin_amdgcn_raw_buffer_load_b32(rws, w < 2 ? (u32)(k * 128) : OOB, WS_CL, AUX_SC1);
    pin();
    asm volatile("s_waitcnt lgkmcnt(0)" : "+s"(E) :: "memory");
    bar();
    FLOOR(5);
    STAMP(1);
    {
        f32x4 acc[8];
#pragma unroll
        for (int j = 0; j < 8; j++) acc[j] = f32x4{0, 0, 0, 0};
        const char* xa = smem + H_XN + (n >> 2) * XN_STRIDE + g * 16;
        gemv8(r0, lds128(xa + b0 * 64), acc);
        gemv8(r1, lds128(xa + b1 * 64), acc);
        if (w < 2) gemv8(r2, lds128(xa + b2 * 64), acc);
        float* red = (float*)(smem + H_RED) + (w * 4 + g) * 128 + 4 * n;
        *(f32x4*)red = f32x4{acc[0][0], acc[1][0], acc[2][0], acc[3][0]};
        *(f32x4*)(red + 64) = f32x4{acc[4][0], acc[5][0], acc[6][0], acc[7][0]};
    }
    asm volatile("" :: "v"(pf), "v"(ptouch));
    STAMP(2);
    bar();
    {
        const float* red = (const float*)(smem + H_RED) + tid;
        float s = 0;
#pragma unroll
        for (int i = 0; i < 8; i++) s += red[i * 512];
        const int b = tid >> 7, idx = tid & 127, d = 8 * ((idx >> 2) & 15) + 4 * (idx >> 6) + (idx & 3);
        *(u16*)(smem + H_QB + b * 320 + d * 2) = (u16)pk2(s, 0.f);
    }
    bar();
    const u32 T = E + 1;
    if (EXPT == 3 && k == 0 && tid == 0) __hip_atomic_store((u64*)(ws + 2048), ((u64)T << 32) | t_fl, __ATOMIC_RELAXED, __HIP_MEMORY_SCOPE_AGENT);
    {
        const char* qa = smem + H_QB + (n & 3) * 320 + ((n >> 2) & 1) * 64 + g * 16;
        const u32x4 q0 = lds128(qa), q1 = lds128(qa + 128);
        f32x4 a0 = mf16(uk[0], q0, f32x4{0, 0, 0, 0}), a1 = mf16(uk[2], q0, f32x4{0, 0, 0, 0});
        a0 = mf16(uk[1], q1, a0); a1 = mf16(uk[3], q1, a1);
        if (n < 8 && (n >> 2) == (g >> 1)) {
            float* st = (float*)(smem + H_ST) + (g >> 1) * 512 + (n & 3) * 128 + 16 * w + 4 * (g & 1);
            *(f32x4*)st = a0; *(f32x4*)(st + 8) = a1;
        }
        if (w < 2) {
            for (int it = 0; it < SPIN_MAX && clf != T; it++) { __builtin_amdgcn_s_sleep(1); clf = __builtin_amdgcn_raw_buffer_load_b32(rws, (u32)(k * 128), WS_CL, AUX_SC1); }
        }
        bar();
        FLOOR(2);
        if (w < 2) {
            const float* st = (const float*)(smem + H_ST) + tid * 4;
            const f32x4 v = *(const f32x4*)st + *(const f32x4*)(st + 512);
            const float sc = 0.08838834764831845f * 1.4426950408889634f;
            const u32 tg = T & 0xffffu;
            u32x4 o;
#pragma unroll
            for (int r = 0; r < 4; r++) o[r] = (pk2(0.f, v[r] * sc) & 0xffff0000u) | tg;
            stb(o, rws, (u32)((((tid >> 5) * 4 + cq) * 32 + h) * 512 + (tid & 31) * 16), WS_QA);
        }
    }
    STAMP(3);
    const u32 tg12 = T & 0xfffu;
    if (k >= 56) {
        const int u = k - 56;
        const bool gu3 = (w & 3) == 0, dn = !gu3 && w != 7;
        const int t = w < 4 ? w - 1 : w - 2;
        const rsrc_t rg = mkrs(w < 4 ? wg : wu, 576 * 4608 * 2);
        const rsrc_t rd = mkrs(gu3 ? (w < 4 ? wg : wu) : wd, 576 * 4608 * 2);
        u32x4 f0[8], f1[8], f2[8], f3[8];
        const u32 vg = (u32)(((n >> 3) * 32 + 8 * g) * 9216 + (n & 7) * 16 + u * 128);
        const u32 vd = (!dn || (t == 4 && n >= 8)) ? OOB : (u32)(8 * g * 1152 + n * 16 + t * 256);
        ld8<AUX_FFN>(f0, rg, vg, (w & 3) * 128 * 9216, 9216);
        ld8<AUX_FFN>(f1, rg, vg, ((w & 3) * 128 + 64) * 9216, 9216);
        ld8<AUX_FFN>(f2, rd, gu3 ? vg : vd, gu3 ? 512 * 9216 : u * 64 * 1152, gu3 ? 9216 : 1152);
        ld8<AUX_FFN>(f3, rd, vd, (u * 64 + 32) * 1152, 1152);
        pin();
        STAMP(10);
        const char* xa = smem + H_XN + (n & 3) * XN_STRIDE + (((n >> 2) & 1) * 32 + 8 * g) * 2;
        float* red = (float*)(smem + H_RED);
        {
            f32x4 acc[8];
#pragma unroll
            for (int j = 0; j < 8; j++) acc[j] = f32x4{0, 0, 0, 0};
            gemv8(f0, lds128(xa + (w & 3) * 256), acc);
            gemv8(f1, lds128(xa + (w & 3) * 256 + 128), acc);
            if (gu3) gemv8(f2, lds128(xa + 1024), acc);
            if ((g & 1) == (n >> 3)) {
                float* p = red + ((w * 2 + (n >> 3)) * 4 + (g >> 1) * 2) * 64 + (n & 7) * 8;
                const bool up = g >= 2;
#pragma unroll
                for (int b = 0; b < 2; b++) {
                    *(f32x4*)(p + b * 64) = up ? f32x4{acc[0][2 + b], acc[1][2 + b], acc[2][2 + b], acc[3][2 + b]} : f32x4{acc[0][b], acc[1][b], acc[2][b], acc[3][b]};
                    *(f32x4*)(p + b * 64 + 4) = up ? f32x4{acc[4][2 + b], acc[5][2 + b], acc[6][2 + b], acc[7][2 + b]} : f32x4{acc[4][b], acc[5][b], acc[6][b], acc[7][b]};
                }
            }
        }
        bar();
        STAMP(11);
        if (tid < 256) {
            float gs = 0, us = 0;
#pragma unroll
            for (int i = 0; i < 8; i++) { gs += red[i * 256 + tid]; us += red[(8 + i) * 256 + tid]; }
            const float gg = rbf(gs), uu = rbf(us);
            const float inner = 0.7978845608028654f * (gg + 0.044715f * gg * gg * gg);
            const float ge = rbf(0.5f * gg * (1.0f + tanhf(inner)));
            *(u16*)(smem + H_HB + (tid >> 6) * 144 + (tid & 63) * 2) = (u16)pk2(ge * uu, 0.f);
        }
        bar();
        if (dn) {
            f32x4 acc[8];
#pragma unroll
            for (int j = 0; j < 8; j++) acc[j] = f32x4{0, 0, 0, 0};
            const char* ha = smem + H_HB + (n >> 2) * 144 + g * 16;
            gemv8(f2, lds128(ha), acc);
            gemv8(f3, lds128(ha + 64), acc);
            if (t < 4 || n < 8) {
                const bool up = (n & 4) != 0;
                u32x4 va, vb;
#pragma unroll
                for (int e = 0; e < 4; e++) {
                    const u32 lo = tag12(acc[e][0], tg12), hi = tag12(acc[4 + e][0], tg12);
                    const u32 fr = __builtin_amdgcn_update_dpp(0, hi, 0x114, 0xf, 0xf, true);
                    const u32 fl = __builtin_amdgcn_update_dpp(0, hi, 0x104, 0xf, 0xf, true);
                    va[e] = up ? fr : lo; vb[e] = up ? lo : fl;
                }
                const u32 vo = (u32)(((g * 18 + 4 * t + 2 * (n >> 3)) * 72 + u) * 128 + (n & 3) * 32);
                stb(va, rws, vo + (up ? 16 : 0), WS_YP);
                stb(vb, rws, vo + 72 * 128 + (up ? 0 : 16), WS_YP);
            }
        }
    }
    STAMP(4);
    if (k >= 56) {
        const int r = k - 56, b = r / 18, m0 = (r - b * 18) * 32;
        float xres = 0.f;
        if (tid < 32) xres = lo2f(x[b * 576 + m0 + tid]);
        f32x4 ys = {0, 0, 0, 0}, ss = {0, 0, 0, 0};
        {
            const u32 vo = (u32)(r * 72 * 128 + tid * 16);
            for (int it = 0; it < SPIN_MAX; it++) {
                u32x4 a = ldb<AUX_SC1>(rws, vo, WS_YP);
                u32x4 d = ldb<AUX_SC1>(rws, tid < 64 ? vo + 8192 : OOB, WS_YP);
                u32 bad = 0;
#pragma unroll
                for (int e = 0; e < 4; e++) bad |= (a[e] ^ tg12) | (tid < 64 ? (d[e] ^ tg12) : 0u);
                if (__all((bad & 0xfffu) == 0) || it == SPIN_MAX - 1) {
#pragma unroll
                    for (int e = 0; e < 4; e++) ys[e] = __uint_as_float(a[e] & 0xfffff000u) + __uint_as_float(d[e] & 0xfffff000u);
                    break;
                }
                __builtin_amdgcn_s_sleep(4);
            }
        }
        STAMP(5);
        {
            const u32 vo = (u32)(r * 128 * 128 + tid * 16);
            for (int it = 0; it < SPIN_MAX; it++) {
                u32x4 a = ldb<AUX_SC1>(rws, vo, WS_SP), c = ldb<AUX_SC1>(rws, vo + 8192, WS_SP);
                u32 bad = 0;
#pragma unroll
                for (int e = 0; e < 4; e++) bad |= (a[e] ^ tg12) | (c[e] ^ tg12);
                if (__all((bad & 0xfffu) == 0) || it == SPIN_MAX - 1) {
#pragma unroll
                    for (int e = 0; e < 4; e++) ss[e] = __uint_as_float(a[e] & 0xfffff000u) + __uint_as_float(c[e] & 0xfffff000u);
                    break;
                }
                __builtin_amdgcn_s_sleep(1);
            }
        }
        STAMP(6);
#pragma unroll
        for (int e = 0; e < 4; e++) {
            ys[e] += dpp<0x128>(ys[e]); ss[e] += dpp<0x128>(ss[e]);
            ys[e] = swap32sum(swap16sum(ys[e])); ss[e] = swap32sum(swap16sum(ss[e]));
        }
        float* fin = (float*)(smem + H_FIN);
        if (l < 8) { *(f32x4*)(fin + w * 32 + l * 4) = ys; *(f32x4*)(fin + 256 + w * 32 + l * 4) = ss; }
        bar();
        if (EXPT == 3) {
            u64 rf = 0;
            for (int it = 0; it < SPIN_MAX; it++) { rf = __hip_atomic_load((const u64*)(ws + 2048), __ATOMIC_RELAXED, __HIP_MEMORY_SCOPE_AGENT); if ((u32)(rf >> 32) == T) break; }
            while ((u32)__builtin_amdgcn_s_memrealtime() - (u32)rf < FLOOR_T) __builtin_amdgcn_s_sleep(2);
        } else
        FLOOR(1);
        if (tid < 32) {
            float y = 0, s = 0;
#pragma unroll
            for (int i = 0; i < 8; i++) { y += fin[i * 32 + tid]; s += fin[256 + i * 32 + tid]; }
            y = rbf(y); s = rbf(s);
            const float gt = rbf(y / (1.0f + __expf(-y)));
            if (EXPT == 2) __hip_atomic_store(out + b * 576 + m0 + tid, (u16)pk2(xres + rbf(s * gt), 0.f), __ATOMIC_RELAXED, __HIP_MEMORY_SCOPE_AGENT);
            else out[b * 576 + m0 + tid] = (u16)pk2(xres + rbf(s * gt), 0.f);
        }
        if (k == 56 && tid == 0) __hip_atomic_store((u32*)ws, T, __ATOMIC_RELAXED, __HIP_MEMORY_SCOPE_AGENT);
        STAMP(7);
    }
    STAMP_FLUSH;
}

#define A_Q 131072
DI void dma_row(const u16* gbase, u32 voff, u32 ldsaddr) {
    u32 keep;
    asm volatile("s_mov_b32 %0, m0\n\ts_mov_b32 m0, %3\n\ts_nop 0\n\tglobal_load_lds_dwordx4 %1, %2" KV_NT "\n\ts_mov_b32 m0, %0"
                 : "=&s"(keep) : "v"(voff), "s"(gbase), "s"(ldsaddr) : "memory");
}
template <int OFF> DI void ld_sc1_asm(u32x4& r, u32 voff, const char* base) {
    asm volatile("global_load_dwordx4 %0, %1, %2 offset:%3 sc1" : "=&v"(r) : "v"(voff), "s"(base), "n"(OFF) : "memory");
}

DI void a_weights(u32x4 (&w0)[8], u32x4 (&w1)[8], u32x4 (&w2)[8], const u16* wuv, const u16* wo, int w, int h, int al, int n, int g) {
    const int kb = w & 3;
    const rsrc_t ro = mkrs(wo, 4096 * 576 * 2);
    const u32 vo = (u32)(8 * g * 1152 + n * 16);
    const u32 so = (u32)((h * 128 + 32 * kb) * 1152);
    if (w < 4) {
        const rsrc_t rv = mkrs(wuv, 512 * 4096 * 2);
        ld8<AUX_WUV>(w0, rv, (u32)(8 * g * 8192 + h * 256 + n * 16), (128 * al + 32 * kb) * 8192, 8192);
    } else {
        ld8<AUX_WO>(w0, ro, vo, so, 1152);
    }
    ld8<AUX_WO>(w1, ro, vo, so + (1 + (w >> 2)) * 256, 1152);
    ld8<AUX_WO>(w2, ro, (w >= 4 && n >= 8) ? OOB : vo, so + (3 + (w >> 2)) * 256, 1152);
}

DI void a_role(char* smem, const u16* ckv, const u16* wuv, const u16* wo, char* ws, u32 pf) {
    const int tid = threadIdx.x, l = tid & 63, w = __builtin_amdgcn_readfirstlane(tid >> 6);
    const int a = blockIdx.x - 128, xc = a & 7, ii = a >> 3;
    const int b = xc >> 1, chunk = (xc & 1) * 16 + ii;
    const int h = 4 * xc + (ii >> 2), al = ii & 3;
    const int n = l & 15, g = l >> 4, hh = l >> 5;
    STAMP_DECL;
    STAMP(0);
    const u32 t_fl = (u32)__builtin_amdgcn_s_memrealtime();
    const rsrc_t rws = mkrs(ws, WS_END);
    u32 E;
    asm volatile("s_load_dword %0, %1, 0x0" : "=s"(E) : "s"(ws) : "memory");
    {
        const u32x4 ones = {~0u, ~0u, ~0u, ~0u};
#pragma unroll
        for (int i = 0; i < 4; i++) stb(ones, rws, (u32)(tid * 16 + i * 8192), WS_PART + (h * 4 + al) * 32768);
        stb(ones, rws, tid < 16 ? (u32)(tid * 16 + a * 256 + WS_ST) : OOB, 0);
    }
    const u32 lds0 = (u32)(size_t)(LAS char*)smem;
    const u16* kb0 = ckv + ((size_t)b * 4096 + chunk * 128 + 16 * w) * 512;
    const u32 ldsw = lds0 + w * 16384;
#pragma unroll
    for (int i = 0; i < CLR_EARLY; i++) dma_row(kb0, (u32)(((l ^ (((i & 3) << 2) | (i >> 2))) << 4) + i * 1024), ldsw + i * 1024);
    asm volatile("s_waitcnt vmcnt(%1) lgkmcnt(0)" : "+s"(E) : "n"(CLR_EARLY), "v"(pf) : "memory");
    STAMP(13);
    const u32 T = E + 1;
    bar();
    STAMP(14);
    if (tid == 0) __builtin_amdgcn_raw_buffer_store_b32(T, rws, (u32)(a * 128 + WS_CL), 0, AUX_SC1);
#pragma unroll
    for (int i = CLR_EARLY; i < 16; i++) dma_row(kb0, (u32)(((l ^ (((i & 3) << 2) | (i >> 2))) << 4) + i * 1024), ldsw + i * 1024);
    u32x4 w0[8], w1[8], w2[8];
    if (DW_EARLY && w < 4) a_weights(w0, w1, w2, wuv, wo, w, h, al, n, g);
    asm volatile("s_waitcnt vmcnt(0)" ::: "memory");
    {
        const char* qsrc = ws + WS_QA + b * 65536 + w * 8192;
        const u32 vq = l * 16 + 4096;
        const u32 tg = T & 0xffffu;
        u32x4 q[8];
        STAMP(1);
        for (int it = 0; it < SPIN_MAX; it++) {
            ld_sc1_asm<-4096>(q[0], vq, qsrc); ld_sc1_asm<-3072>(q[1], vq, qsrc);
            ld_sc1_asm<-2048>(q[2], vq, qsrc); ld_sc1_asm<-1024>(q[3], vq, qsrc);
            ld_sc1_asm<0>(q[4], vq, qsrc); ld_sc1_asm<1024>(q[5], vq, qsrc);
            ld_sc1_asm<2048>(q[6], vq, qsrc); ld_sc1_asm<3072>(q[7], vq, qsrc);
            asm volatile("s_waitcnt vmcnt(0)" : "+v"(q[0]), "+v"(q[1]), "+v"(q[2]), "+v"(q[3]), "+v"(q[4]), "+v"(q[5]), "+v"(q[6]), "+v"(q[7]) :: "memory");
            u32 bad = 0;
#pragma unroll
            for (int i = 0; i < 8; i++) bad |= (q[i][0] ^ tg) | (q[i][1] ^ tg) | (q[i][2] ^ tg) | (q[i][3] ^ tg);
            if (__all((bad & 0xffffu) == 0)) break;
            __builtin_amdgcn_s_sleep(POLL_SLEEP);
        }
        STAMP(2);
        const int z = (l >> 1) & 7;
        char* qd = smem + A_Q + (8 * (w >> 1) + ((l & 31) >> 2)) * 1024 + ((l >> 1) & 1) * 512 + (l & 1) * 8;
#pragma unroll
        for (int i = 0; i < 8; i++) {
            const int hq = 16 * (w & 1) + 2 * i + hh;
            u32x2 o = {__builtin_amdgcn_perm(q[i][1], q[i][0], 0x07060302u), __builtin_amdgcn_perm(q[i][3], q[i][2], 0x07060302u)};
            *(u32x2*)(qd + ((hq ^ z) << 4)) = o;
        }
    }
    bar();
    FLOOR(3);
    STAMP(3);
    if (!DW_EARLY || w >= 4) { a_weights(w0, w1, w2, wuv, wo, w, h, al, n, g); pin(); }
    f32x16 sacc;
    float mx = 0.f;
    if (w < 4) {
#pragma unroll
        for (int r = 0; r < 16; r++) sacc[r] = 0.f;
        const int row = 32 * w + (l & 31);
        const int f = ((row & 3) << 2) | ((row >> 2) & 3);
        const char* kp = smem + row * 1024;
        const int fx = (f ^ hh) << 4;
        const char* qp = smem + A_Q + hh * 512;
#pragma unroll
        for (int s = 0; s < 32; s++) {
            const u32x4 ka = lds128(kp + ((32 * (s & 7)) ^ fx) + 256 * (s >> 3));
            const u32x4 qb = lds128(qp + s * 1024 + (((l & 31) ^ ((s & 3) * 2 + hh)) << 4));
            sacc = mf32(ka, qb, sacc);
        }
        mx = sacc[0];
#pragma unroll
        for (int r = 1; r < 16; r++) mx = fmaxf(mx, sacc[r]);
        mx = swap32max(mx);
    }
    bar();
    float* aux = (float*)(smem + A_Q);
    if (w < 4 && l < 32) aux[w * 32 + l] = mx;
    bar();
    if (w < 4) {
        const int hq = l & 31;
        const float M = fmaxf(fmaxf(aux[hq], aux[32 + hq]), fmaxf(aux[64 + hq], aux[96 + hq]));
        float p[16], ls = 0.f;
#pragma unroll
        for (int r = 0; r < 16; r++) { p[r] = __builtin_amdgcn_exp2f(sacc[r] - M); ls += p[r]; }
        ls = swap32sum(ls);
        if (l < 32) aux[128 + w * 32 + l] = ls;
        char* pd = smem + A_Q + 1024 + (2 * w) * 1024 + l * 16;
        *(u32x4*)pd = u32x4{pk2(p[0], p[1]), pk2(p[2], p[3]), pk2(p[4], p[5]), pk2(p[6], p[7])};
        *(u32x4*)(pd + 1024) = u32x4{pk2(p[8], p[9]), pk2(p[10], p[11]), pk2(p[12], p[13]), pk2(p[14], p[15])};
    }
    bar();
    STAMP(4);
    if (w == 4 && l < 16) {
        u32x4 o;
#pragma unroll
        for (int e = 0; e < 2; e++) {
            const int hq = 2 * l + e;
            const float M = fmaxf(fmaxf(aux[hq], aux[32 + hq]), fmaxf(aux[64 + hq], aux[96 + hq]));
            const float L = (aux[128 + hq] + aux[160 + hq]) + (aux[192 + hq] + aux[224 + hq]);
            o[2 * e] = min(__float_as_uint(M), 0xfffffffeu); o[2 * e + 1] = min(__float_as_uint(L), 0xfffffffeu);
        }
        stb(o, rws, (u32)((b * 32 + chunk) * 256 + l * 16), WS_ST);
    }
    {
        f32x16 o0, o1;
#pragma unroll
        for (int r = 0; r < 16; r++) { o0[r] = 0.f; o1[r] = 0.f; }
        const int q4 = (l & 15) >> 2, p4 = l & 3, blk = (l >> 4) & 1;
        const char* vb = smem + (4 * hh + q4) * 1024 + (w >> 1) * 256 + (p4 >> 1) * 8;
        const int cl = 8 * (w & 1) + 2 * blk + (p4 & 1);
        const char* pp = smem + A_Q + 1024 + l * 16;
#pragma unroll
        for (int ks = 0; ks < 8; ks++) {
            const u32x4 pb = lds128(pp + ks * 1024);
            u32x4 va[2];
#pragma unroll
            for (int cb = 0; cb < 2; cb++) {
#pragma unroll
                for (int t = 0; t < 2; t++) {
                    const int f = (q4 << 2) | (hh + 2 * t);
                    const char* ad = vb + ks * 16384 + t * 8192 + (((cl + 4 * cb) ^ f) << 4);
                    s16x4 v = __builtin_amdgcn_ds_read_tr16_b64_v4i16((LAS s16x4*)ad);
                    u32x2 vv = __builtin_bit_cast(u32x2, v);
                    va[cb][2 * t] = vv[0]; va[cb][2 * t + 1] = vv[1];
                }
            }
            o0 = mf32(va[0], pb, o0);
            o1 = mf32(va[1], pb, o1);
        }
        STAMP(5);
        u32x4 s0[2], s1[2];
#pragma unroll
        for (int i = 0; i < 2; i++) {
#pragma unroll
            for (int e = 0; e < 4; e++) {
                s0[i][e] = min(pk2(o0[8 * i + 2 * e], o0[8 * i + 2 * e + 1]), 0xfffffffeu);
                s1[i][e] = min(pk2(o1[8 * i + 2 * e], o1[8 * i + 2 * e + 1]), 0xfffffffeu);
            }
        }
        char* stg = smem + A_Q + 9216 + w * 2304;
        char* sw = stg + (l & 15) * 144 + hh * 16;
        const char* sr = stg + (l >> 3) * 144 + (l & 7) * 16;
        const u32 vo = (u32)((((l >> 3) * 4 + (w >> 1)) * 128 + b * 32 + chunk) * 256 + (w & 1) * 128 + (l & 7) * 16);
#pragma unroll
        for (int rd = 0; rd < 2; rd++) {
            if (((l >> 4) & 1) == rd) {
                *(u32x4*)(sw) = s0[0]; *(u32x4*)(sw + 32) = s0[1]; *(u32x4*)(sw + 64) = s1[0]; *(u32x4*)(sw + 96) = s1[1];
            }
            asm volatile("" ::: "memory");
            const u32x4 v0 = lds128(sr), v1 = lds128(sr + 8 * 144);
            stb(v0, rws, vo + (16 * rd) * 131072, WS_PART);
            stb(v1, rws, vo + (16 * rd + 8) * 131072, WS_PART);
            asm volatile("" ::: "memory");
        }
    }
    STAMP(6);
    u32x4 t0[8], t1[8], t2[8];
    tr8(w0, t0); tr8(w1, t1); tr8(w2, t2);
#pragma unroll
    for (int j = 0; j < 8; j++) asm volatile("" : "+v"(t0[j]), "+v"(t1[j]), "+v"(t2[j]));
    STAMP(7);
#define D_WT (A_Q + 28672)
#define D_OB 4096
#define D_CX 8192
#define D_SR 16384
    u32x4 pt[8];
    if (w < 4) {
        const u32 vo = (u32)((w * 32 + 8 * g) * 256 + n * 16);
        const u32 so = WS_PART + (h * 4 + al) * 32768;
        u32x4 pr[8];
        for (int it = 0; it < SPIN_MAX; it++) {
#pragma unroll
            for (int i = 0; i < 8; i++) pr[i] = ldb<AUX_SC1>(rws, vo + i * 256, so);
            u32 mxv = 0;
#pragma unroll
            for (int i = 0; i < 8; i++) mxv = max(max(mxv, max(pr[i][0], pr[i][1])), max(pr[i][2], pr[i][3]));
            if (__all(mxv != 0xffffffffu)) break;
            __builtin_amdgcn_s_sleep(1);
        }
        tr8(pr, pt);
    }
    if (w == 4 || w == 5) {
        const u32 vo = (u32)(((2 * (w - 4) + hh) * 32 + (l & 31)) * 256 + h * 8);
        u32x2 sr;
        for (int it = 0; it < SPIN_MAX; it++) {
            sr = __builtin_amdgcn_raw_buffer_load_b64(rws, vo, WS_ST, AUX_SC1);
            if (__all(max(sr[0], sr[1]) != 0xffffffffu)) break;
            __builtin_amdgcn_s_sleep(1);
        }
        const float m = __uint_as_float(sr[0]);
        float M = m;
        M = fmaxf(M, dpp<0xb1>(M)); M = fmaxf(M, dpp<0x4e>(M)); M = fmaxf(M, dpp<0x141>(M)); M = fmaxf(M, dpp<0x140>(M)); M = swap16max(M);
        const float e = __builtin_amdgcn_exp2f(m - M);
        float L = e * __uint_as_float(sr[1]);
        L += dpp<0xb1>(L); L += dpp<0x4e>(L); L += dpp<0x141>(L); L += dpp<0x140>(L); L = swap16sum(L);
        const float a = e / L;
        const u32 hi = pk2(a, 0.f);
        const u32 lo = pk2(a - lo2f(hi), 0.f);
        u16* wt = (u16*)(smem + D_WT) + (2 * (w - 4) + hh) * 64 + (l & 31);
        wt[0] = (u16)hi; wt[32] = (u16)lo;
    }
    bar();
    FLOOR(4);
    STAMP(8);
    if (w < 4) {
        u32x4 aw = lds128(smem + D_WT + w * 128 + (n & 1) * 64 + g * 16);
        if (n >= 2) aw = u32x4{0, 0, 0, 0};
        float o[8];
#pragma unroll
        for (int j = 0; j < 8; j++) { f32x4 d = mf16(aw, pt[j], f32x4{0, 0, 0, 0}); o[j] = d[0] + d[1]; }
        if (g == 0) *(u32x4*)(smem + D_OB + w * 320 + n * 16) = u32x4{pk2(o[0], o[1]), pk2(o[2], o[3]), pk2(o[4], o[5]), pk2(o[6], o[7])};
    }
    bar();
    if (w < 4) {
        const u32x4 ao = lds128(smem + D_OB + (n >> 2) * 320 + (32 * w + 8 * g) * 2);
        f32x4 d[8];
#pragma unroll
        for (int j = 0; j < 8; j++) d[j] = mf16(ao, t0[j], f32x4{0, 0, 0, 0});
        float* cx = (float*)(smem + D_CX) + (w * 4 + g) * 128 + 8 * n;
        *(f32x4*)cx = f32x4{d[0][0], d[1][0], d[2][0], d[3][0]};
        *(f32x4*)(cx + 4) = f32x4{d[4][0], d[5][0], d[6][0], d[7][0]};
    }
    bar();
    {
        const int kb = w & 3;
        const float* cx = (const float*)(smem + D_CX) + (n >> 2) * 128 + 32 * kb + 8 * g;
        f32x4 lo = *(const f32x4*)cx, hi = *(const f32x4*)(cx + 4);
#pragma unroll
        for (int i = 1; i < 4; i++) { lo += *(const f32x4*)(cx + i * 512); hi += *(const f32x4*)(cx + i * 512 + 4); }
        const u32x4 ac = {pk2(lo[0], lo[1]), pk2(lo[2], lo[3]), pk2(hi[0], hi[1]), pk2(hi[2], hi[3])};
        float* sr = (float*)(smem + D_SR) + (kb * 4 + g) * 640 + 8 * n;
        f32x4 d[8];
        if (w >= 4) {
#pragma unroll
            for (int j = 0; j < 8; j++) d[j] = mf16(ac, t0[j], f32x4{0, 0, 0, 0});
            *(f32x4*)sr = f32x4{d[0][0], d[1][0], d[2][0], d[3][0]};
            *(f32x4*)(sr + 4) = f32x4{d[4][0], d[5][0], d[6][0], d[7][0]};
        }
#pragma unroll
        for (int j = 0; j < 8; j++) d[j] = mf16(ac, t1[j], f32x4{0, 0, 0, 0});
        float* s1 = sr + (1 + (w >> 2)) * 128;
        *(f32x4*)s1 = f32x4{d[0][0], d[1][0], d[2][0], d[3][0]};
        *(f32x4*)(s1 + 4) = f32x4{d[4][0], d[5][0], d[6][0], d[7][0]};
#pragma unroll
        for (int j = 0; j < 8; j++) d[j] = mf16(ac, t2[j], f32x4{0, 0, 0, 0});
        float* s2 = sr + (3 + (w >> 2)) * 128;
        *(f32x4*)s2 = f32x4{d[0][0], d[1][0], d[2][0], d[3][0]};
        *(f32x4*)(s2 + 4) = f32x4{d[4][0], d[5][0], d[6][0], d[7][0]};
    }
    bar();
    {
        const u32 tg12 = T & 0xfffu;
#pragma unroll
        for (int i = 0; i < 2; i++) {
            const int ch = tid + 512 * i;
            if (ch < 576) {
                const int bb = ch / 144, m = (ch - bb * 144) * 4;
                const float* sr = (const float*)(smem + D_SR) + bb * 640 + m;
                f32x4 s = *(const f32x4*)sr;
#pragma unroll
                for (int q = 1; q < 4; q++) s += *(const f32x4*)(sr + q * 2560);
                stb(u32x4{tag12(s[0], tg12), tag12(s[1], tg12), tag12(s[2], tg12), tag12(s[3], tg12)}, rws,
                    (u32)(((bb * 18 + (m >> 5)) * 128 + a) * 128 + (m & 31) * 4), WS_SP);
            }
        }
    }
    STAMP(9);
    STAMP_FLUSH;
}

#define IPF 0
template <int V>
__global__ __launch_bounds__(512, 2) void fused_decode(const u16* x, const u16* ckv, const u16* wq, const u16* wuk, const u16* wuv,
                                                       const u16* wo, const u16* wg, const u16* wu, const u16* wd, u16* out, char* ws) {
    extern __shared__ __attribute__((aligned(16))) char smem[];
    u32 pf = 0;
    if (blockIdx.x < 128) h_role(smem, x, wq, wuk, wg, wu, wd, out, ws, pf);
    else a_role(smem, ckv, wuv, wo, ws, pf);
}

extern "C" void kernel_launch(void* const* d_in, const int* in_sizes, int n_in, void* d_out, int out_size, void* d_ws, size_t ws_size,
                              hipStream_t stream) {
    (void)in_sizes; (void)n_in; (void)out_size; (void)ws_size;
    hipFuncSetAttribute((const void*)fused_decode<0>, hipFuncAttributeMaxDynamicSharedMemorySize, 163840);
    fused_decode<0><<<256, 512, 163840, stream>>>((const u16*)d_in[0], (const u16*)d_in[1], (const u16*)d_in[2], (const u16*)d_in[3],
                                                  (const u16*)d_in[4], (const u16*)d_in[5], (const u16*)d_in[7], (const u16*)d_in[8],
                                                  (const u16*)d_in[9], (u16*)d_out, (char*)d_ws);
}
```
